```python
import torch

def _softmax(x, axis=-1):
    return torch.softmax(x, dim=axis)

def _gelu(x):
    return torch.nn.functional.gelu(x, approximate='tanh')

def _silu(x):
    return torch.nn.functional.silu(x)

def _sigmoid(x):
    return torch.sigmoid(x)

def _layernorm(x, axis=-1, eps=1e-05):
    m = x.mean(dim=axis, keepdim=True)
    v = x.var(dim=axis, keepdim=True, unbiased=False)
    return (x - m) / torch.sqrt(v + eps)

def _scatter_add(tgt, idx, src, axis=0):
    return tgt.clone().index_add_(axis, idx, src)

def _segment_sum(src, seg, n):
    out = torch.zeros((n,) + src.shape[1:], dtype=src.dtype)
    return out.index_add_(0, seg, src)

def _pad(x, pw):
    flat = tuple((v for pair in reversed(pw) for v in pair))
    return torch.nn.functional.pad(x, flat)
kernel_name = 'sequential_preLN__memory_aug_recurrent_mem_tr__dense_activation_relu__prompt'

def setup_inputs():

    _w = lambda a: (lambda t: t.to(torch.bfloat16) if t.is_floating_point() else t)(torch.as_tensor(a))

    def _A_setup():
        B, S, Dm = (1, 2048, 640)
        H, Dh = (32, 128)
        M, seg = (8, 32)
        seg = min(seg, S)
        S = S // seg * seg
        return {'x': _w((lambda *s: torch.randn(*s, dtype=torch.bfloat16) * 0.05)(B, S, Dm)), 'mem0': _w((lambda *s: torch.randn(*s, dtype=torch.bfloat16) * 0.2)(B, M, Dm)), 'Wq': _w((lambda *s: torch.randn(*s, dtype=torch.bfloat16) * 1.6)(Dm, H, Dh) * Dm ** (-0.5)), 'Wk': _w((lambda *s: torch.randn(*s, dtype=torch.bfloat16) * 1.6)(Dm, H, Dh) * Dm ** (-0.5)), 'Wv': _w((lambda *s: torch.randn(*s, dtype=torch.bfloat16) * 0.2)(Dm, H, Dh) * Dm ** (-0.5)), 'Wo': _w((lambda *s: torch.randn(*s, dtype=torch.bfloat16) * 0.2)(H, Dh, Dm) * (H * Dh) ** (-0.5)), 'seg_len': seg}
    _A_in = _A_setup()

    def _B_setup():
        B, S, D, Dff = (1, 2048, 640, 1728)
        return {'x': _w((lambda *s: torch.randn(*s, dtype=torch.bfloat16) * 0.05)(B, S, D)), 'w1': _w((lambda *s: torch.randn(*s, dtype=torch.bfloat16) * 0.05)(D, Dff)), 'w2': _w((lambda *s: torch.randn(*s, dtype=torch.bfloat16) * 0.05)(Dff, D))}
    _B_in = _B_setup()
    return {'A_x': _A_in['x'], 'A_mem0': _A_in['mem0'], 'A_Wq': _A_in['Wq'], 'A_Wk': _A_in['Wk'], 'A_Wv': _A_in['Wv'], 'A_Wo': _A_in['Wo'], 'A_seg_len': _A_in['seg_len'], 'B_x': _B_in['x'], 'B_w1': _B_in['w1'], 'B_w2': _B_in['w2']}

def reference(A_x, A_mem0, A_Wq, A_Wk, A_Wv, A_Wo, A_seg_len, B_x, B_w1, B_w2):

    def _A_ref(x, mem0, Wq, Wk, Wv, Wo, seg_len):
        B, S, Dm = x.shape
        H, Dh = (Wq.shape[1], Wq.shape[2])
        M = mem0.shape[1]
        scale = Dh ** (-0.5)
        n_seg = S // seg_len
        L = M + seg_len
        idx = torch.arange(L)
        mask = (idx[:, None] < M) | (idx[:, None] >= idx[None, :])
        mem = mem0
        outs = []
        for s in range(n_seg):
            xs = x[:, s * seg_len:(s + 1) * seg_len, :].clone()
            h = torch.concatenate([mem, xs], axis=1)
            q = torch.einsum('btd,dhe->bhte', h, Wq)
            k = torch.einsum('btd,dhe->bhte', h, Wk)
            v = torch.einsum('btd,dhe->bhte', h, Wv)
            scores = torch.einsum('bhqe,bhke->bhqk', q, k) * scale
            scores = torch.where(mask[None, None], scores, -1000000000.0)
            p = _softmax(scores, axis=-1)
            o = torch.einsum('bhqk,bhke->bhqe', p, v)
            o = torch.einsum('bhte,hed->btd', o, Wo)
            mem = o[:, :M, :].clone()
            outs.append(o[:, M:, :])
        return torch.concatenate(outs, axis=1)

    def _B_ref(x, w1, w2):
        h = torch.einsum('bsd,df->bsf', x, w1)
        zero = torch.zeros_like(h)
        h = torch.maximum(h, zero)
        return torch.einsum('bsf,fd->bsd', h, w2)
    x = A_x
    h = x + _A_ref(_layernorm(x, axis=-1), A_mem0, A_Wq, A_Wk, A_Wv, A_Wo, A_seg_len)
    h2 = h + _B_ref(_layernorm(h, axis=-1), B_w1, B_w2)
    return h2
```

```cpp
#include <hip/hip_runtime.h>
#include <hip/hip_bf16.h>
#include <stdint.h>
typedef unsigned short u16;
typedef __bf16 bf8 __attribute__((ext_vector_type(8)));
typedef float f4 __attribute__((ext_vector_type(4)));
__device__ __forceinline__ float bf2f(u16 v) { return __uint_as_float((unsigned)v << 16); }
__device__ __forceinline__ u16 f2bf(float f) { __bf16 b = (__bf16)f; return __builtin_bit_cast(u16, b); }

constexpr int S = 2048, D = 640, H = 32, E = 128, HE = 4096, M = 8, SEG = 32, NSEG = 64, FF = 1728;

__global__ void k_ln(const u16* x, u16* y) {
    int row = blockIdx.x * 4 + (threadIdx.x >> 6), l = threadIdx.x & 63;
    float v[10]; float s = 0;
    for (int i = 0; i < 10; ++i) { v[i] = bf2f(x[row * D + l + 64 * i]); s += v[i]; }
    for (int o = 32; o; o >>= 1) s += __shfl_xor(s, o);
    float m = s / D, q = 0;
    for (int i = 0; i < 10; ++i) { float d = v[i] - m; q += d * d; }
    for (int o = 32; o; o >>= 1) q += __shfl_xor(q, o);
    float r = 1.0f / sqrtf(q / D + 1e-5f);
    for (int i = 0; i < 10; ++i) y[row * D + l + 64 * i] = f2bf((v[i] - m) * r);
}
template <int MODE>
__global__ void __launch_bounds__(256) k_gemm(const u16* A, int lda, const u16* W, int ldw, u16* C, int ldc, const u16* res, int K, int Mrows) {
    __shared__ u16 As[64][40];
    __shared__ u16 Ws[64][40];
    int m0 = blockIdx.y * 64, n0 = blockIdx.x * 64, t = threadIdx.x, w = t >> 6, l = t & 63;
    int wm = (w >> 1) * 32, wn = (w & 1) * 32;
    f4 acc[2][2] = {};
    for (int k0 = 0; k0 < K; k0 += 32) {
        for (int i = t; i < 64 * 32; i += 256) { int r = i >> 5, c = i & 31; As[r][c] = (m0 + r < Mrows) ? A[(size_t)(m0 + r) * lda + k0 + c] : 0; }
        for (int i = t; i < 64 * 32; i += 256) { int k = i >> 6, n = i & 63; Ws[n][k] = W[(size_t)(k0 + k) * ldw + n0 + n]; }
        __syncthreads();
        bf8 a[2], b[2];
        for (int i = 0; i < 2; ++i) {
            a[i] = *(const bf8*)&As[wm + i * 16 + (l & 15)][(l >> 4) * 8];
            b[i] = *(const bf8*)&Ws[wn + i * 16 + (l & 15)][(l >> 4) * 8];
        }
        for (int i = 0; i < 2; ++i) for (int j = 0; j < 2; ++j) acc[i][j] = __builtin_amdgcn_mfma_f32_16x16x32_bf16(a[i], b[j], acc[i][j], 0, 0, 0);
        __syncthreads();
    }
    for (int i = 0; i < 2; ++i) for (int j = 0; j < 2; ++j) for (int r = 0; r < 4; ++r) {
        int m = m0 + wm + i * 16 + (l >> 4) * 4 + r, n = n0 + wn + j * 16 + (l & 15);
        if (m >= Mrows) continue;
        float v = acc[i][j][r];
        if (MODE == 1) v = fmaxf(v, 0.f);
        if (MODE == 2) v = bf2f(f2bf(v)) + bf2f(res[(size_t)m * ldc + n]);
        C[(size_t)m * ldc + n] = f2bf(v);
    }
}
template <bool MEMQ>
__global__ void __launch_bounds__(256) k_attn(const u16* q, const u16* kmem, const u16* vmem, const u16* kseg, const u16* vseg, u16* o, int seg0) {
    int h = blockIdx.x, s = seg0 + blockIdx.y, t = threadIdx.x;
    constexpr int NQ = MEMQ ? 8 : 32;
    __shared__ float sq[NQ][E], sk[40][E + 1], sv[40][E], p[NQ][40];
    const u16* qp = MEMQ ? q + (size_t)s * M * HE : q + (size_t)s * SEG * HE;
    for (int i = t; i < NQ * E; i += 256) sq[i / E][i % E] = bf2f(qp[(size_t)(i / E) * HE + h * E + i % E]);
    for (int i = t; i < 40 * E; i += 256) {
        int r = i / E, e = i % E;
        sk[r][e] = bf2f(r < 8 ? kmem[((size_t)s * M + r) * HE + h * E + e] : kseg[((size_t)s * SEG + r - 8) * HE + h * E + e]);
        sv[r][e] = bf2f(r < 8 ? vmem[((size_t)s * M + r) * HE + h * E + e] : vseg[((size_t)s * SEG + r - 8) * HE + h * E + e]);
    }
    __syncthreads();
    for (int i = t; i < NQ * 40; i += 256) {
        int r = i / 40, c = i % 40; float a = 0;
        for (int e = 0; e < E; ++e) a += sq[r][e] * sk[c][e];
        a *= 0.08838834764831845f;
        if (!MEMQ && c > r + 8) a = -1e9f;
        p[r][c] = a;
    }
    __syncthreads();
    if (t < NQ) {
        float m = -3e38f; for (int c = 0; c < 40; ++c) m = fmaxf(m, p[t][c]);
        float z = 0; for (int c = 0; c < 40; ++c) { float e = __expf(p[t][c] - m); p[t][c] = e; z += e; }
        for (int c = 0; c < 40; ++c) p[t][c] = bf2f(f2bf(p[t][c] / z));
    }
    __syncthreads();
    u16* op = MEMQ ? o : o + (size_t)s * SEG * HE;
    for (int i = t; i < NQ * E; i += 256) {
        int r = i / E, e = i % E; float a = 0;
        for (int c = 0; c < 40; ++c) a += p[r][c] * sv[c][e];
        op[(size_t)r * HE + h * E + e] = f2bf(a);
    }
}

extern "C" void kernel_launch(void* const* d_in, const int* in_sizes, int n_in, void* d_out, int out_size, void* d_ws, size_t ws_size, hipStream_t stream) {
    const u16* x = (const u16*)d_in[0]; const u16* mem0 = (const u16*)d_in[1];
    const u16* Wq = (const u16*)d_in[2]; const u16* Wk = (const u16*)d_in[3]; const u16* Wv = (const u16*)d_in[4]; const u16* Wo = (const u16*)d_in[5];
    const u16* w1 = (const u16*)d_in[8]; const u16* w2 = (const u16*)d_in[9];
    u16* ws = (u16*)d_ws;
    u16* xn = ws; ws += S * D;
    u16* Q = ws; ws += (size_t)S * HE; u16* Kb = ws; ws += (size_t)S * HE; u16* V = ws; ws += (size_t)S * HE; u16* O = ws; ws += (size_t)S * HE;
    u16* mem = ws; ws += NSEG * M * D;
    u16* qm = ws; ws += NSEG * M * HE; u16* km = ws; ws += NSEG * M * HE; u16* vm = ws; ws += NSEG * M * HE;
    u16* om = ws; ws += M * HE;
    u16* hb = ws; ws += S * D; u16* hn = ws; ws += S * D; u16* u = ws; ws += S * FF;
    k_ln<<<S / 4, 256, 0, stream>>>(x, xn);
    k_gemm<0><<<dim3(HE / 64, S / 64), 256, 0, stream>>>(xn, D, Wq, HE, Q, HE, nullptr, D, S);
    k_gemm<0><<<dim3(HE / 64, S / 64), 256, 0, stream>>>(xn, D, Wk, HE, Kb, HE, nullptr, D, S);
    k_gemm<0><<<dim3(HE / 64, S / 64), 256, 0, stream>>>(xn, D, Wv, HE, V, HE, nullptr, D, S);
    hipMemcpyAsync(mem, mem0, M * D * 2, hipMemcpyDeviceToDevice, stream);
    for (int s = 0; s < NSEG; ++s) {
        const u16* ms = mem + s * M * D;
        k_gemm<0><<<dim3(HE / 64, 1), 256, 0, stream>>>(ms, D, Wk, HE, km + (size_t)s * M * HE, HE, nullptr, D, M);
        k_gemm<0><<<dim3(HE / 64, 1), 256, 0, stream>>>(ms, D, Wv, HE, vm + (size_t)s * M * HE, HE, nullptr, D, M);
        if (s == NSEG - 1) break;
        k_gemm<0><<<dim3(HE / 64, 1), 256, 0, stream>>>(ms, D, Wq, HE, qm + (size_t)s * M * HE, HE, nullptr, D, M);
        k_attn<true><<<dim3(H, 1), 256, 0, stream>>>(qm, km, vm, Kb, V, om, s);
        k_gemm<0><<<dim3(D / 64, 1), 256, 0, stream>>>(om, HE, Wo, D, mem + (s + 1) * M * D, D, nullptr, HE, M);
    }
    k_attn<false><<<dim3(H, NSEG), 256, 0, stream>>>(Q, km, vm, Kb, V, O, 0);
    k_gemm<2><<<dim3(D / 64, S / 64), 256, 0, stream>>>(O, HE, Wo, D, hb, D, x, HE, S);
    k_ln<<<S / 4, 256, 0, stream>>>(hb, hn);
    k_gemm<1><<<dim3(FF / 64, S / 64), 256, 0, stream>>>(hn, D, w1, FF, u, FF, nullptr, D, S);
    k_gemm<2><<<dim3(D / 64, S / 64), 256, 0, stream>>>(u, FF, w2, D, (u16*)d_out, D, hb, FF, S);
}
```

```cpp
#include <hip/hip_runtime.h>
#include <hip/hip_bf16.h>
#include <stdint.h>

typedef unsigned short u16;
typedef unsigned u32;
typedef unsigned v4u __attribute__((ext_vector_type(4)));
typedef unsigned v2u __attribute__((ext_vector_type(2)));
typedef float f4 __attribute__((ext_vector_type(4)));
typedef float f2 __attribute__((ext_vector_type(2)));
typedef __bf16 b2 __attribute__((ext_vector_type(2)));
typedef __bf16 b4 __attribute__((ext_vector_type(4)));
typedef __bf16 b8 __attribute__((ext_vector_type(8)));
typedef __bf16 b16 __attribute__((ext_vector_type(16)));
#define LAS __attribute__((address_space(3)))
#define GAS __attribute__((address_space(1)))
#define DEV __device__ __forceinline__
typedef __amdgpu_buffer_rsrc_t rsrc_t;

constexpr int S_ = 2048, D_ = 640, H_ = 32, HE_ = 4096, NSEG = 64, FF_ = 1728;
constexpr u32 MB = 1u << 20;
constexpr u32 WS_CTL = 0, WS_WINA = 2 * MB, WS_WINB = 4 * MB, WS_XN = 6 * MB, WS_KF = 10 * MB, WS_VF = 27 * MB, WS_KVM = 44 * MB,
              WS_OF = 53 * MB, WS_HB = 70 * MB, WS_HN = 73 * MB, WS_UF = 76 * MB, WS_WP = 84 * MB, WS_DBG = 128 * MB;
constexpr u32 C_TICKET = 0, C_OVF = 2048, C_SEL = 2304, C_ERR = 2560, C_KVP = 16384, C_CHP = 32768, C_QAP = 49152, C_WOP = 53248, C_RDP = 53504, C_F1P = 53760, C_LNP = 54016, C_BYTES = 65536;
constexpr int SC1 = 16;
#define STAUX 16
#define LDAUX 16
constexpr u32 SPIN_CAP = 1u << 19;

struct Args { const u16 *x, *mem0, *Wq, *Wk, *Wv, *Wo, *w1, *w2; u16* out; unsigned char* ws; u32 dl, pad; };
constexpr u32 g_dl = 0x1a0a0421u;

DEV float bf2f(u32 v) { return __uint_as_float(v << 16); }
DEV u32 pk2(float a, float b) { f2 p = {a, b}; return __builtin_bit_cast(u32, __builtin_convertvector(p, b2)); }
DEV v4u lds_r128(u32 o) { return *(LAS v4u*)(uintptr_t)o; }
DEV void lds_w128(u32 o, v4u v) { *(LAS v4u*)(uintptr_t)o = v; }
DEV void lds_w64(u32 o, v2u v) { *(LAS v2u*)(uintptr_t)o = v; }
DEV b4 lds_tr(u32 o) { return __builtin_amdgcn_ds_read_tr16_b64_v4bf16((LAS b4*)(uintptr_t)o); }
DEV b8 cat(b4 a, b4 b) { return __builtin_shufflevector(a, b, 0, 1, 2, 3, 4, 5, 6, 7); }
DEV b16 cat(b8 a, b8 b) { return __builtin_shufflevector(a, b, 0, 1, 2, 3, 4, 5, 6, 7, 8, 9, 10, 11, 12, 13, 14, 15); }
DEV b8 lo8(b16 a) { return __builtin_shufflevector(a, a, 0, 1, 2, 3, 4, 5, 6, 7); }
DEV b8 hi8(b16 a) { return __builtin_shufflevector(a, a, 8, 9, 10, 11, 12, 13, 14, 15); }
DEV b8 asb8(v4u v) { return __builtin_bit_cast(b8, v); }
DEV f4 mfma(b8 a, b8 b, f4 c) { return __builtin_amdgcn_mfma_f32_16x16x32_bf16(a, b, c, 0, 0, 0); }
DEV f4 smf(b8 a, b16 b, f4 c, int idx) { return __builtin_amdgcn_smfmac_f32_16x16x64_bf16(a, b, c, idx, 0, 0); }
DEV void glds16(const void* g, u32 ldsoff) { __builtin_amdgcn_global_load_lds((const GAS void*)g, (LAS void*)(uintptr_t)ldsoff, 16, 0, 0); }
DEV void wdma(rsrc_t r, u32 vo, u32 so, u32 lds) { asm volatile("s_mov_b32 m0, %3\n\ts_nop 0\n\tbuffer_load_dwordx4 %0, %1, %2 offen lds" :: "v"(vo), "s"(r), "s"(so), "s"(lds) : "memory"); }
DEV void wdmas(rsrc_t r, u32 vo, u32 so, u32 lds) { asm volatile("s_mov_b32 m0, %3\n\ts_nop 0\n\tbuffer_load_dwordx4 %0, %1, %2 offen sc1 lds" :: "v"(vo), "s"(r), "s"(so), "s"(lds) : "memory"); }
DEV void gdma(const void* sb, u32 vo, u32 lds) { asm volatile("s_mov_b32 m0, %2\n\ts_nop 0\n\tglobal_load_lds_dwordx4 %0, %1" :: "v"(vo), "s"(sb), "s"(lds) : "memory"); }
DEV v4u alda(const void* sb, u32 vo) { v4u r; asm volatile("global_load_dwordx4 %0, %1, %2" : "=a"(r) : "v"(vo), "s"(sb) : "memory"); return r; }
DEV v4u gld128(rsrc_t r, u32 vo, u32 so) { return __builtin_amdgcn_raw_buffer_load_b128(r, vo, so, LDAUX); }
DEV u32 gld32(rsrc_t r, u32 vo, u32 so) { return __builtin_amdgcn_raw_buffer_load_b32(r, vo, so, SC1); }
DEV void gst128(rsrc_t r, v4u v, u32 vo, u32 so) { __builtin_amdgcn_raw_buffer_store_b128(v, r, vo, so, STAUX); asm volatile("s_nop 1" :: "v"(v)); }
DEV void gst32(rsrc_t r, u32 v, u32 vo, u32 so) { __builtin_amdgcn_raw_buffer_store_b32(v, r, vo, so, SC1); }
#define WAIT_V(n) asm volatile("s_waitcnt vmcnt(" #n ")" ::: "memory")
#define WAIT_L0() asm volatile("s_waitcnt lgkmcnt(0)" ::: "memory")
#define BAR() do { asm volatile("s_waitcnt lgkmcnt(0)" ::: "memory"); __builtin_amdgcn_s_barrier(); asm volatile("" ::: "memory"); } while (0)
#define SB() __builtin_amdgcn_sched_barrier(0)
#define MEMFENCE() asm volatile("" ::: "memory")
DEV float asf(u32 v) { return __builtin_bit_cast(float, v); }
DEV u32 asu(float v) { return __builtin_bit_cast(u32, v); }
DEV float sum32(float a, float b) { auto r = __builtin_amdgcn_permlane32_swap(asu(a), asu(b), false, false); return asf(r[0]) + asf(r[1]); }
DEV float sum16(float a, float b) { auto r = __builtin_amdgcn_permlane16_swap(asu(a), asu(b), false, false); return asf(r[0]) + asf(r[1]); }
DEV float allmax_g(float m) {
    auto r = __builtin_amdgcn_permlane32_swap(asu(m), asu(m), false, false); m = fmaxf(asf(r[0]), asf(r[1]));
    auto q = __builtin_amdgcn_permlane16_swap(asu(m), asu(m), false, false); return fmaxf(asf(q[0]), asf(q[1]));
}
DEV float allsum_g(float m) { m = sum32(m, m); return sum16(m, m); }
DEV unsigned long long rtime() { return __builtin_amdgcn_s_memrealtime(); }
#define CALN 3
#define PULL 128
#define HPERM 1
#define D1 0
#define D2 0

__global__ void __launch_bounds__(1024) k_pro(Args a) {
    const int b = (int)blockIdx.x - 8, t = threadIdx.x, l = t & 63, w = t >> 6;
    __shared__ u32 cal[16];
    if (b >= 0) {
        const u32 id = b * 1024 + t;
        const v4u ff = {~0u, ~0u, ~0u, ~0u}, z = {0, 0, 0, 0};
        if (id < 40960) *(v4u*)(a.ws + WS_WINA + id * 16) = ff;
        else if (id < 122880) {
            const u32 j = id - 40960, sl = j / 1280, r = j % 1280;
            *(v4u*)(a.ws + WS_WINB + sl * 32768 + (r < 1024 ? r * 16 : 16384 + ((r - 1024) >> 7) * 8192 + (r & 127) * 16)) = ff;
        } else { const u32 o = (id - 122880) * 16; if (o < C_BYTES && (o & ~63u) != C_SEL) *(v4u*)(a.ws + WS_CTL + o) = z; }
    } else {
        const rsrc_t rs = __builtin_amdgcn_make_buffer_rsrc(a.ws, 0, 0x7FFFFFFF, 0x00020000);
        const u32 x = __builtin_amdgcn_s_getreg(20 | (3 << 11)) & 7u;
        const u32 base = ((w & 2) ? WS_WINB + 20480 + (b + 8) * 32768 : WS_WINA + (48 + b + 8) * 16384) + (w & 1) * 8192 + (w >> 2) * 512;
        u32 best = 0xffffu;
        for (int i = 0; i < CALN; ++i) {
            unsigned long long t0, t1;
            asm volatile("s_waitcnt vmcnt(0)\n\ts_memtime %0\n\ts_waitcnt lgkmcnt(0)" : "=s"(t0) :: "memory");
            const v4u z = {0, 0, 0, 0};
            if (l < 8) gst128(rs, z, base + i * 128 + l * 16, 0);
            asm volatile("s_waitcnt vmcnt(0)\n\ts_memtime %0\n\ts_waitcnt lgkmcnt(0)" : "=s"(t1) :: "memory");
            const u32 d = (u32)(t1 - t0); if (d < best) best = d;
        }
        if (l == 0) cal[w ^ (x >> 2)] = best;
        __syncthreads();
        if (t == 0) {
            u32 c[4];
            for (int i = 0; i < 4; ++i) c[i] = min(min(cal[i], cal[i + 4]), min(cal[i + 8], cal[i + 12]));
            u32* o = (u32*)(a.ws + WS_CTL + C_SEL) + b + 8; o[0] = c[0] | (c[1] << 16); o[8] = c[2] | (c[3] << 16);
        }
    }
}

DEV u32 flag_base(const Args& a) {
    SB();
    const __attribute__((address_space(4))) u32* c = (const __attribute__((address_space(4))) u32*)(uintptr_t)(a.ws + WS_CTL + C_SEL);
    u32 m0 = ~0u, m1 = ~0u;
#pragma unroll
    for (int y = 0; y < 8; ++y) { m0 = min(m0, c[y] & 0xffffu); m1 = min(m1, c[y] >> 16); }
    return WS_CTL + (m1 < m0 ? 8192u : 0u);
}
template <int PERM> DEV int rho2row(int rho) {
    if (PERM == 0) return (rho & 0x31) | ((rho >> 2) & 2) | ((rho << 1) & 0xC);
    if (PERM == 1) return (rho & 0x33) | ((rho >> 1) & 4) | ((rho << 1) & 8);
    return rho;
}
template <int PERM> DEV void tile_offs(u32 (&vo)[4], int w, int l, int ldw_bytes) {
#pragma unroll
    for (int i = 0; i < 4; ++i) {
        const int rho = 16 * w + 4 * i + (l >> 4), ch = l & 15;
        vo[i] = rho2row<PERM>(rho) * ldw_bytes + (((ch >> 1) ^ (rho & 7)) * 32) + (ch & 1) * 16;
    }
}
DEV void stage_tile(const unsigned char* src, const u32 (&vo)[4], u32 slot, int w) {
#pragma unroll
    for (int i = 0; i < 4; ++i) gdma(src, vo[i], slot + (w * 4 + i) * 1024);
}
#define WAIT_A8(n, R, o) asm volatile("s_waitcnt vmcnt(" #n ")" : "+a"(R[o]), "+a"(R[o + 1]), "+a"(R[o + 2]), "+a"(R[o + 3]), "+a"(R[o + 4]), "+a"(R[o + 5]), "+a"(R[o + 6]), "+a"(R[o + 7]) :: "memory")

constexpr u32 L_MEM = 114688, L_KB = 124928, L_VB = 133120, L_QK = 149504;
#define CHAIN_ASM_3 \
    "s_load_dwordx16 s[68:83], s[8:9], 0x900\n" \
    "global_load_dwordx4 v[72:75], v91, s[20:21]\n" \
    "global_load_dwordx4 v[76:79], v91, s[20:21] offset:128\n" \
    "global_load_dwordx4 v[80:83], v91, s[20:21] offset:256\n" \
    "s_add_u32 s40, s14, 0\n" \
    "s_addc_u32 s41, s15, 0\n" \
    "s_add_u32 m0, s22, 65536\n" \
    "s_nop 0\n" \
    "global_load_lds_dwordx4 v84, s[40:41]\n" \
    "s_add_u32 m0, s22, 66560\n" \
    "s_nop 0\n" \
    "global_load_lds_dwordx4 v85, s[40:41]\n" \
    "s_add_u32 m0, s22, 67584\n" \
    "s_nop 0\n" \
    "global_load_lds_dwordx4 v86, s[40:41]\n" \
    "s_add_u32 m0, s22, 68608\n" \
    "s_nop 0\n" \
    "global_load_lds_dwordx4 v87, s[40:41]\n" \
    "s_add_u32 s40, s16, 0\n" \
    "s_addc_u32 s41, s17, 0\n" \
    "s_add_u32 m0, s22, 81920\n" \
    "s_nop 0\n" \
    "global_load_lds_dwordx4 v84, s[40:41]\n" \
    "s_add_u32 m0, s22, 82944\n" \
    "s_nop 0\n" \
    "global_load_lds_dwordx4 v85, s[40:41]\n" \
    "s_add_u32 m0, s22, 83968\n" \
    "s_nop 0\n" \
    "global_load_lds_dwordx4 v86, s[40:41]\n" \
    "s_add_u32 m0, s22, 84992\n" \
    "s_nop 0\n" \
    "global_load_lds_dwordx4 v87, s[40:41]\n" \
    "s_add_u32 s40, s14, 524288\n" \
    "s_addc_u32 s41, s15, 0\n" \
    "s_add_u32 m0, s22, 98304\n" \
    "s_nop 0\n" \
    "global_load_lds_dwordx4 v84, s[40:41]\n" \
    "s_add_u32 m0, s22, 99328\n" \
    "s_nop 0\n" \
    "global_load_lds_dwordx4 v85, s[40:41]\n" \
    "s_add_u32 m0, s22, 100352\n" \
    "s_nop 0\n" \
    "global_load_lds_dwordx4 v86, s[40:41]\n" \
    "s_add_u32 m0, s22, 101376\n" \
    "s_nop 0\n" \
    "global_load_lds_dwordx4 v87, s[40:41]\n" \
    "s_add_u32 s40, s16, 524288\n" \
    "s_addc_u32 s41, s17, 0\n" \
    "s_add_u32 m0, s22, 114688\n" \
    "s_nop 0\n" \
    "global_load_lds_dwordx4 v84, s[40:41]\n" \
    "s_add_u32 m0, s22, 115712\n" \
    "s_nop 0\n" \
    "global_load_lds_dwordx4 v85, s[40:41]\n" \
    "s_add_u32 m0, s22, 116736\n" \
    "s_nop 0\n" \
    "global_load_lds_dwordx4 v86, s[40:41]\n" \
    "s_add_u32 m0, s22, 117760\n" \
    "s_nop 0\n" \
    "global_load_lds_dwordx4 v87, s[40:41]\n" \
    "s_add_u32 s40, s16, 1048576\n" \
    "s_addc_u32 s41, s17, 0\n" \
    "s_add_u32 m0, s22, 0\n" \
    "s_nop 0\n" \
    "global_load_lds_dwordx4 v84, s[40:41]\n" \
    "s_add_u32 m0, s22, 1024\n" \
    "s_nop 0\n" \
    "global_load_lds_dwordx4 v85, s[40:41]\n" \
    "s_add_u32 m0, s22, 2048\n" \
    "s_nop 0\n" \
    "global_load_lds_dwordx4 v86, s[40:41]\n" \
    "s_add_u32 m0, s22, 3072\n" \
    "s_nop 0\n" \
    "global_load_lds_dwordx4 v87, s[40:41]\n" \
    "s_add_u32 s40, s14, 1572864\n" \
    "s_addc_u32 s41, s15, 0\n" \
    "s_add_u32 m0, s22, 16384\n" \
    "s_nop 0\n" \
    "global_load_lds_dwordx4 v84, s[40:41]\n" \
    "s_add_u32 m0, s22, 17408\n" \
    "s_nop 0\n" \
    "global_load_lds_dwordx4 v85, s[40:41]\n" \
    "s_add_u32 m0, s22, 18432\n" \
    "s_nop 0\n" \
    "global_load_lds_dwordx4 v86, s[40:41]\n" \
    "s_add_u32 m0, s22, 19456\n" \
    "s_nop 0\n" \
    "global_load_lds_dwordx4 v87, s[40:41]\n" \
    "s_add_u32 s40, s16, 1572864\n" \
    "s_addc_u32 s41, s17, 0\n" \
    "s_add_u32 m0, s22, 32768\n" \
    "s_nop 0\n" \
    "global_load_lds_dwordx4 v84, s[40:41]\n" \
    "s_add_u32 m0, s22, 33792\n" \
    "s_nop 0\n" \
    "global_load_lds_dwordx4 v85, s[40:41]\n" \
    "s_add_u32 m0, s22, 34816\n" \
    "s_nop 0\n" \
    "global_load_lds_dwordx4 v86, s[40:41]\n" \
    "s_add_u32 m0, s22, 35840\n" \
    "s_nop 0\n" \
    "global_load_lds_dwordx4 v87, s[40:41]\n" \
    "s_add_u32 s40, s16, 2097152\n" \
    "s_addc_u32 s41, s17, 0\n" \
    "s_add_u32 m0, s22, 49152\n" \
    "s_nop 0\n" \
    "global_load_lds_dwordx4 v84, s[40:41]\n" \
    "s_add_u32 m0, s22, 50176\n" \
    "s_nop 0\n" \
    "global_load_lds_dwordx4 v85, s[40:41]\n" \
    "s_add_u32 m0, s22, 51200\n" \
    "s_nop 0\n" \
    "global_load_lds_dwordx4 v86, s[40:41]\n" \
    "s_add_u32 m0, s22, 52224\n" \
    "s_nop 0\n" \
    "global_load_lds_dwordx4 v87, s[40:41]\n" \
    "s_waitcnt lgkmcnt(0)\n" \
    "s_and_b32 s40, s68, 0xffff\n" \
    "s_lshr_b32 s41, s68, 16\n" \
    "s_and_b32 s46, s69, 0xffff\n" \
    "s_min_u32 s40, s40, s46\n" \
    "s_lshr_b32 s46, s69, 16\n" \
    "s_min_u32 s41, s41, s46\n" \
    "s_and_b32 s46, s70, 0xffff\n" \
    "s_min_u32 s40, s40, s46\n" \
    "s_lshr_b32 s46, s70, 16\n" \
    "s_min_u32 s41, s41, s46\n" \
    "s_and_b32 s46, s71, 0xffff\n" \
    "s_min_u32 s40, s40, s46\n" \
    "s_lshr_b32 s46, s71, 16\n" \
    "s_min_u32 s41, s41, s46\n" \
    "s_and_b32 s46, s72, 0xffff\n" \
    "s_min_u32 s40, s40, s46\n" \
    "s_lshr_b32 s46, s72, 16\n" \
    "s_min_u32 s41, s41, s46\n" \
    "s_and_b32 s46, s73, 0xffff\n" \
    "s_min_u32 s40, s40, s46\n" \
    "s_lshr_b32 s46, s73, 16\n" \
    "s_min_u32 s41, s41, s46\n" \
    "s_and_b32 s46, s74, 0xffff\n" \
    "s_min_u32 s40, s40, s46\n" \
    "s_lshr_b32 s46, s74, 16\n" \
    "s_min_u32 s41, s41, s46\n" \
    "s_and_b32 s46, s75, 0xffff\n" \
    "s_min_u32 s40, s40, s46\n" \
    "s_lshr_b32 s46, s75, 16\n" \
    "s_min_u32 s41, s41, s46\n" \
    "s_cmp_lt_u32 s41, s40\n" \
    "s_cselect_b32 s40, 0x2000, 0\n" \
    "v_or_b32 v9, s40, v9\n" \
    "s_or_b32 s24, s24, s40\n" \
    "s_or_b32 s25, s25, s40\n" \
    "s_and_b32 s40, s76, 0xffff\n" \
    "s_lshr_b32 s41, s76, 16\n" \
    "s_and_b32 s46, s77, 0xffff\n" \
    "s_min_u32 s40, s40, s46\n" \
    "s_lshr_b32 s46, s77, 16\n" \
    "s_min_u32 s41, s41, s46\n" \
    "s_and_b32 s46, s78, 0xffff\n" \
    "s_min_u32 s40, s40, s46\n" \
    "s_lshr_b32 s46, s78, 16\n" \
    "s_min_u32 s41, s41, s46\n" \
    "s_and_b32 s46, s79, 0xffff\n" \
    "s_min_u32 s40, s40, s46\n" \
    "s_lshr_b32 s46, s79, 16\n" \
    "s_min_u32 s41, s41, s46\n" \
    "s_and_b32 s46, s80, 0xffff\n" \
    "s_min_u32 s40, s40, s46\n" \
    "s_lshr_b32 s46, s80, 16\n" \
    "s_min_u32 s41, s41, s46\n" \
    "s_and_b32 s46, s81, 0xffff\n" \
    "s_min_u32 s40, s40, s46\n" \
    "s_lshr_b32 s46, s81, 16\n" \
    "s_min_u32 s41, s41, s46\n" \
    "s_and_b32 s46, s82, 0xffff\n" \
    "s_min_u32 s40, s40, s46\n" \
    "s_lshr_b32 s46, s82, 16\n" \
    "s_min_u32 s41, s41, s46\n" \
    "s_and_b32 s46, s83, 0xffff\n" \
    "s_min_u32 s40, s40, s46\n" \
    "s_lshr_b32 s46, s83, 16\n" \
    "s_min_u32 s41, s41, s46\n" \
    "s_cmp_lt_u32 s41, s40\n" \
    "s_cselect_b32 s40, 0x2000, 0\n" \
    "v_or_b32 v10, s40, v10\n" \
    "v_add_u32 v92, 0x10000, v88\n" \
    "v_add_u32 v93, 0x10000, v89\n" \
    "v_mov_b32 v32, 0\n" \
    "v_mov_b32 v33, 0\n" \
    "v_mov_b32 v34, 0\n" \
    "v_mov_b32 v35, 0\n" \
    "ds_write_b128 v1, v[32:35] offset:40960\n" \
    "v_mov_b32 v36, 0x3f803f80\n" \
    "v_mov_b32 v37, 0x3f803f80\n" \
    "v_mov_b32 v38, 0x3f803f80\n" \
    "v_mov_b32 v39, 0x3f803f80\n" \
    "ds_write_b128 v1, v[36:39] offset:41984\n" \
    "v_mov_b32 v40, 0x3f003f00\n" \
    "v_mov_b32 v41, 0x3f003f00\n" \
    "v_mov_b32 v42, 0x3f003f00\n" \
    "v_mov_b32 v43, 0x3f003f00\n" \
    "ds_write_b128 v1, v[40:43] offset:43008\n" \
    "v_lshrrev_b32 v48, 4, v1\n" \
    "v_and_b32 v49, 1, v48\n" \
    "v_lshlrev_b32 v49, 4, v49\n" \
    "v_mov_b32 v50, 0x3f80\n" \
    "v_lshlrev_b32 v50, v49, v50\n" \
    "v_bfe_u32 v48, v48, 1, 2\n" \
    "v_cmp_eq_u32 vcc, 0, v48\n" \
    "s_nop 1\n" \
    "v_cndmask_b32 v44, 0, v50, vcc\n" \
    "v_cmp_eq_u32 vcc, 1, v48\n" \
    "s_nop 1\n" \
    "v_cndmask_b32 v45, 0, v50, vcc\n" \
    "v_cmp_eq_u32 vcc, 2, v48\n" \
    "s_nop 1\n" \
    "v_cndmask_b32 v46, 0, v50, vcc\n" \
    "v_cmp_eq_u32 vcc, 3, v48\n" \
    "s_nop 1\n" \
    "v_cndmask_b32 v47, 0, v50, vcc\n" \
    "ds_write_b128 v1, v[44:47] offset:44032\n" \
    "s_mov_b32 s27, 0x3e0293ee\n" \
    "s_mov_b32 s28, 0\n" \
    "s_mov_b32 s29, 0xa40000\n" \
    "s_mov_b32 s30, 0x1b80000\n" \
    "s_mov_b32 s31, 0x408000\n" \
    "s_mov_b32 s34, 0x2c00000\n" \
    "s_mov_b32 s35, 0\n" \
    "s_mov_b32 s36, 0\n" \
    "s_add_u32 s37, s22, 133120\n" \
    "s_add_u32 s43, s22, 124928\n" \
    "s_mov_b32 s38, 0\n" \
    "s_mov_b32 s39, 0\n" \
    "s_mov_b32 s44, 0x00ffffff\n" \
    "s_mov_b32 s45, 0x00ffffff\n" \
    "s_mov_b32 s42, 2\n" \
    "s_waitcnt vmcnt(16)\n" \
    "s_waitcnt lgkmcnt(0)\n" \
    "s_barrier\n" \
    "ds_read_b64_tr_b16 a[0:1], v92\n" \
    "ds_read_b64_tr_b16 a[2:3], v92 offset:2048\n" \
    "ds_read_b64_tr_b16 a[4:5], v92 offset:8192\n" \
    "ds_read_b64_tr_b16 a[6:7], v92 offset:10240\n" \
    "ds_read_b64_tr_b16 a[8:9], v93\n" \
    "ds_read_b64_tr_b16 a[10:11], v93 offset:2048\n" \
    "ds_read_b64_tr_b16 a[12:13], v93 offset:8192\n" \
    "ds_read_b64_tr_b16 a[14:15], v93 offset:10240\n" \
    "ds_read_b64_tr_b16 a[16:17], v92 offset:16384\n" \
    "ds_read_b64_tr_b16 a[18:19], v92 offset:18432\n" \
    "ds_read_b64_tr_b16 a[20:21], v92 offset:24576\n" \
    "ds_read_b64_tr_b16 a[22:23], v92 offset:26624\n" \
    "ds_read_b64_tr_b16 a[24:25], v93 offset:16384\n" \
    "ds_read_b64_tr_b16 a[26:27], v93 offset:18432\n" \
    "ds_read_b64_tr_b16 a[28:29], v93 offset:24576\n" \
    "ds_read_b64_tr_b16 a[30:31], v93 offset:26624\n" \
    "ds_read_b64_tr_b16 a[48:49], v92 offset:32768\n" \
    "ds_read_b64_tr_b16 a[50:51], v92 offset:34816\n" \
    "ds_read_b64_tr_b16 a[52:53], v92 offset:40960\n" \
    "ds_read_b64_tr_b16 a[54:55], v92 offset:43008\n" \
    "ds_read_b64_tr_b16 a[56:57], v93 offset:32768\n" \
    "ds_read_b64_tr_b16 a[58:59], v93 offset:34816\n" \
    "ds_read_b64_tr_b16 a[60:61], v93 offset:40960\n" \
    "ds_read_b64_tr_b16 a[62:63], v93 offset:43008\n" \
    "ds_read_b64_tr_b16 a[64:65], v92 offset:49152\n" \
    "ds_read_b64_tr_b16 a[66:67], v92 offset:51200\n" \
    "ds_read_b64_tr_b16 a[68:69], v92 offset:57344\n" \
    "ds_read_b64_tr_b16 a[70:71], v92 offset:59392\n" \
    "ds_read_b64_tr_b16 a[72:73], v93 offset:49152\n" \
    "ds_read_b64_tr_b16 a[74:75], v93 offset:51200\n" \
    "ds_read_b64_tr_b16 a[76:77], v93 offset:57344\n" \
    "ds_read_b64_tr_b16 a[78:79], v93 offset:59392\n" \
    "s_waitcnt lgkmcnt(0)\n" \
    "s_barrier\n" \
    "s_add_u32 s40, s14, 2621440\n" \
    "s_addc_u32 s41, s15, 0\n" \
    "s_add_u32 m0, s22, 65536\n" \
    "s_nop 0\n" \
    "global_load_lds_dwordx4 v84, s[40:41]\n" \
    "s_add_u32 m0, s22, 66560\n" \
    "s_nop 0\n" \
    "global_load_lds_dwordx4 v85, s[40:41]\n" \
    "s_add_u32 m0, s22, 67584\n" \
    "s_nop 0\n" \
    "global_load_lds_dwordx4 v86, s[40:41]\n" \
    "s_add_u32 m0, s22, 68608\n" \
    "s_nop 0\n" \
    "global_load_lds_dwordx4 v87, s[40:41]\n" \
    "s_add_u32 s40, s16, 2621440\n" \
    "s_addc_u32 s41, s17, 0\n" \
    "s_add_u32 m0, s22, 81920\n" \
    "s_nop 0\n" \
    "global_load_lds_dwordx4 v84, s[40:41]\n" \
    "s_add_u32 m0, s22, 82944\n" \
    "s_nop 0\n" \
    "global_load_lds_dwordx4 v85, s[40:41]\n" \
    "s_add_u32 m0, s22, 83968\n" \
    "s_nop 0\n" \
    "global_load_lds_dwordx4 v86, s[40:41]\n" \
    "s_add_u32 m0, s22, 84992\n" \
    "s_nop 0\n" \
    "global_load_lds_dwordx4 v87, s[40:41]\n" \
    "s_add_u32 s40, s14, 3145728\n" \
    "s_addc_u32 s41, s15, 0\n" \
    "s_add_u32 m0, s22, 98304\n" \
    "s_nop 0\n" \
    "global_load_lds_dwordx4 v84, s[40:41]\n" \
    "s_add_u32 m0, s22, 99328\n" \
    "s_nop 0\n" \
    "global_load_lds_dwordx4 v85, s[40:41]\n" \
    "s_add_u32 m0, s22, 100352\n" \
    "s_nop 0\n" \
    "global_load_lds_dwordx4 v86, s[40:41]\n" \
    "s_add_u32 m0, s22, 101376\n" \
    "s_nop 0\n" \
    "global_load_lds_dwordx4 v87, s[40:41]\n" \
    "s_add_u32 s40, s16, 3145728\n" \
    "s_addc_u32 s41, s17, 0\n" \
    "s_add_u32 m0, s22, 114688\n" \
    "s_nop 0\n" \
    "global_load_lds_dwordx4 v84, s[40:41]\n" \
    "s_add_u32 m0, s22, 115712\n" \
    "s_nop 0\n" \
    "global_load_lds_dwordx4 v85, s[40:41]\n" \
    "s_add_u32 m0, s22, 116736\n" \
    "s_nop 0\n" \
    "global_load_lds_dwordx4 v86, s[40:41]\n" \
    "s_add_u32 m0, s22, 117760\n" \
    "s_nop 0\n" \
    "global_load_lds_dwordx4 v87, s[40:41]\n" \
    "s_waitcnt vmcnt(16)\n" \
    "s_waitcnt lgkmcnt(0)\n" \
    "s_barrier\n" \
    "ds_read_b64_tr_b16 a[96:97], v88\n" \
    "ds_read_b64_tr_b16 a[98:99], v88 offset:2048\n" \
    "ds_read_b64_tr_b16 a[100:101], v88 offset:8192\n" \
    "ds_read_b64_tr_b16 a[102:103], v88 offset:10240\n" \
    "ds_read_b64_tr_b16 a[104:105], v89\n" \
    "ds_read_b64_tr_b16 a[106:107], v89 offset:2048\n" \
    "ds_read_b64_tr_b16 a[108:109], v89 offset:8192\n" \
    "ds_read_b64_tr_b16 a[110:111], v89 offset:10240\n" \
    "ds_read_b64_tr_b16 a[112:113], v88 offset:16384\n" \
    "ds_read_b64_tr_b16 a[114:115], v88 offset:18432\n" \
    "ds_read_b64_tr_b16 a[116:117], v88 offset:24576\n" \
    "ds_read_b64_tr_b16 a[118:119], v88 offset:26624\n" \
    "ds_read_b64_tr_b16 a[120:121], v89 offset:16384\n" \
    "ds_read_b64_tr_b16 a[122:123], v89 offset:18432\n" \
    "ds_read_b64_tr_b16 a[124:125], v89 offset:24576\n" \
    "ds_read_b64_tr_b16 a[126:127], v89 offset:26624\n" \
    "ds_read_b64_tr_b16 a[128:129], v88 offset:32768\n" \
    "ds_read_b64_tr_b16 a[130:131], v88 offset:34816\n" \
    "ds_read_b64_tr_b16 a[132:133], v88 offset:40960\n" \
    "ds_read_b64_tr_b16 a[134:135], v88 offset:43008\n" \
    "ds_read_b64_tr_b16 a[136:137], v89 offset:32768\n" \
    "ds_read_b64_tr_b16 a[138:139], v89 offset:34816\n" \
    "ds_read_b64_tr_b16 a[140:141], v89 offset:40960\n" \
    "ds_read_b64_tr_b16 a[142:143], v89 offset:43008\n" \
    "ds_read_b64_tr_b16 a[160:161], v88 offset:49152\n" \
    "ds_read_b64_tr_b16 a[162:163], v88 offset:51200\n" \
    "ds_read_b64_tr_b16 a[164:165], v88 offset:57344\n" \
    "ds_read_b64_tr_b16 a[166:167], v88 offset:59392\n" \
    "ds_read_b64_tr_b16 a[168:169], v89 offset:49152\n" \
    "ds_read_b64_tr_b16 a[170:171], v89 offset:51200\n" \
    "ds_read_b64_tr_b16 a[172:173], v89 offset:57344\n" \
    "ds_read_b64_tr_b16 a[174:175], v89 offset:59392\n" \
    "s_waitcnt lgkmcnt(0)\n" \
    "s_barrier\n" \
    "s_add_u32 s40, s16, 3670016\n" \
    "s_addc_u32 s41, s17, 0\n" \
    "s_add_u32 m0, s22, 0\n" \
    "s_nop 0\n" \
    "global_load_lds_dwordx4 v84, s[40:41]\n" \
    "s_add_u32 m0, s22, 1024\n" \
    "s_nop 0\n" \
    "global_load_lds_dwordx4 v85, s[40:41]\n" \
    "s_add_u32 m0, s22, 2048\n" \
    "s_nop 0\n" \
    "global_load_lds_dwordx4 v86, s[40:41]\n" \
    "s_add_u32 m0, s22, 3072\n" \
    "s_nop 0\n" \
    "global_load_lds_dwordx4 v87, s[40:41]\n" \
    "s_add_u32 s40, s16, 4194304\n" \
    "s_addc_u32 s41, s17, 0\n" \
    "s_add_u32 m0, s22, 16384\n" \
    "s_nop 0\n" \
    "global_load_lds_dwordx4 v84, s[40:41]\n" \
    "s_add_u32 m0, s22, 17408\n" \
    "s_nop 0\n" \
    "global_load_lds_dwordx4 v85, s[40:41]\n" \
    "s_add_u32 m0, s22, 18432\n" \
    "s_nop 0\n" \
    "global_load_lds_dwordx4 v86, s[40:41]\n" \
    "s_add_u32 m0, s22, 19456\n" \
    "s_nop 0\n" \
    "global_load_lds_dwordx4 v87, s[40:41]\n" \
    "s_add_u32 s40, s14, 4718592\n" \
    "s_addc_u32 s41, s15, 0\n" \
    "s_add_u32 m0, s22, 32768\n" \
    "s_nop 0\n" \
    "global_load_lds_dwordx4 v84, s[40:41]\n" \
    "s_add_u32 m0, s22, 33792\n" \
    "s_nop 0\n" \
    "global_load_lds_dwordx4 v85, s[40:41]\n" \
    "s_add_u32 m0, s22, 34816\n" \
    "s_nop 0\n" \
    "global_load_lds_dwordx4 v86, s[40:41]\n" \
    "s_add_u32 m0, s22, 35840\n" \
    "s_nop 0\n" \
    "global_load_lds_dwordx4 v87, s[40:41]\n" \
    "s_add_u32 s40, s16, 4718592\n" \
    "s_addc_u32 s41, s17, 0\n" \
    "s_add_u32 m0, s22, 49152\n" \
    "s_nop 0\n" \
    "global_load_lds_dwordx4 v84, s[40:41]\n" \
    "s_add_u32 m0, s22, 50176\n" \
    "s_nop 0\n" \
    "global_load_lds_dwordx4 v85, s[40:41]\n" \
    "s_add_u32 m0, s22, 51200\n" \
    "s_nop 0\n" \
    "global_load_lds_dwordx4 v86, s[40:41]\n" \
    "s_add_u32 m0, s22, 52224\n" \
    "s_nop 0\n" \
    "global_load_lds_dwordx4 v87, s[40:41]\n" \
    "s_waitcnt vmcnt(16)\n" \
    "s_waitcnt lgkmcnt(0)\n" \
    "s_barrier\n" \
    "ds_read_b64_tr_b16 a[192:193], v92\n" \
    "ds_read_b64_tr_b16 a[194:195], v92 offset:2048\n" \
    "ds_read_b64_tr_b16 a[196:197], v92 offset:8192\n" \
    "ds_read_b64_tr_b16 a[198:199], v92 offset:10240\n" \
    "ds_read_b64_tr_b16 a[200:201], v93\n" \
    "ds_read_b64_tr_b16 a[202:203], v93 offset:2048\n" \
    "ds_read_b64_tr_b16 a[204:205], v93 offset:8192\n" \
    "ds_read_b64_tr_b16 a[206:207], v93 offset:10240\n" \
    "ds_read_b64_tr_b16 a[208:209], v92 offset:16384\n" \
    "ds_read_b64_tr_b16 a[210:211], v92 offset:18432\n" \
    "ds_read_b64_tr_b16 a[212:213], v92 offset:24576\n" \
    "ds_read_b64_tr_b16 a[214:215], v92 offset:26624\n" \
    "ds_read_b64_tr_b16 a[216:217], v93 offset:16384\n" \
    "ds_read_b64_tr_b16 a[218:219], v93 offset:18432\n" \
    "ds_read_b64_tr_b16 a[220:221], v93 offset:24576\n" \
    "ds_read_b64_tr_b16 a[222:223], v93 offset:26624\n" \
    "ds_read_b64_tr_b16 a[224:225], v92 offset:32768\n" \
    "ds_read_b64_tr_b16 a[226:227], v92 offset:34816\n" \
    "ds_read_b64_tr_b16 a[228:229], v92 offset:40960\n" \
    "ds_read_b64_tr_b16 a[230:231], v92 offset:43008\n" \
    "ds_read_b64_tr_b16 a[232:233], v93 offset:32768\n" \
    "ds_read_b64_tr_b16 a[234:235], v93 offset:34816\n" \
    "ds_read_b64_tr_b16 a[236:237], v93 offset:40960\n" \
    "ds_read_b64_tr_b16 a[238:239], v93 offset:43008\n" \
    "ds_read_b64_tr_b16 a[240:241], v92 offset:49152\n" \
    "ds_read_b64_tr_b16 a[242:243], v92 offset:51200\n" \
    "ds_read_b64_tr_b16 a[244:245], v92 offset:57344\n" \
    "ds_read_b64_tr_b16 a[246:247], v92 offset:59392\n" \
    "ds_read_b64_tr_b16 a[248:249], v93 offset:49152\n" \
    "ds_read_b64_tr_b16 a[250:251], v93 offset:51200\n" \
    "ds_read_b64_tr_b16 a[252:253], v93 offset:57344\n" \
    "ds_read_b64_tr_b16 a[254:255], v93 offset:59392\n" \
    "s_waitcnt lgkmcnt(0)\n" \
    "s_barrier\n" \
    "s_add_u32 s40, s12, 524288\n" \
    "s_addc_u32 s41, s13, 0\n" \
    "s_add_u32 m0, s22, 65536\n" \
    "s_nop 0\n" \
    "global_load_lds_dwordx4 v84, s[40:41]\n" \
    "s_add_u32 m0, s22, 66560\n" \
    "s_nop 0\n" \
    "global_load_lds_dwordx4 v85, s[40:41]\n" \
    "s_add_u32 m0, s22, 67584\n" \
    "s_nop 0\n" \
    "global_load_lds_dwordx4 v86, s[40:41]\n" \
    "s_add_u32 m0, s22, 68608\n" \
    "s_nop 0\n" \
    "global_load_lds_dwordx4 v87, s[40:41]\n" \
    "s_add_u32 s40, s12, 1048576\n" \
    "s_addc_u32 s41, s13, 0\n" \
    "s_add_u32 m0, s22, 81920\n" \
    "s_nop 0\n" \
    "global_load_lds_dwordx4 v84, s[40:41]\n" \
    "s_add_u32 m0, s22, 82944\n" \
    "s_nop 0\n" \
    "global_load_lds_dwordx4 v85, s[40:41]\n" \
    "s_add_u32 m0, s22, 83968\n" \
    "s_nop 0\n" \
    "global_load_lds_dwordx4 v86, s[40:41]\n" \
    "s_add_u32 m0, s22, 84992\n" \
    "s_nop 0\n" \
    "global_load_lds_dwordx4 v87, s[40:41]\n" \
    "s_add_u32 s40, s12, 2097152\n" \
    "s_addc_u32 s41, s13, 0\n" \
    "s_add_u32 m0, s22, 98304\n" \
    "s_nop 0\n" \
    "global_load_lds_dwordx4 v84, s[40:41]\n" \
    "s_add_u32 m0, s22, 99328\n" \
    "s_nop 0\n" \
    "global_load_lds_dwordx4 v85, s[40:41]\n" \
    "s_add_u32 m0, s22, 100352\n" \
    "s_nop 0\n" \
    "global_load_lds_dwordx4 v86, s[40:41]\n" \
    "s_add_u32 m0, s22, 101376\n" \
    "s_nop 0\n" \
    "global_load_lds_dwordx4 v87, s[40:41]\n" \
    "s_add_u32 s40, s12, 2621440\n" \
    "s_addc_u32 s41, s13, 0\n" \
    "s_add_u32 m0, s22, 114688\n" \
    "s_nop 0\n" \
    "global_load_lds_dwordx4 v84, s[40:41]\n" \
    "s_add_u32 m0, s22, 115712\n" \
    "s_nop 0\n" \
    "global_load_lds_dwordx4 v85, s[40:41]\n" \
    "s_add_u32 m0, s22, 116736\n" \
    "s_nop 0\n" \
    "global_load_lds_dwordx4 v86, s[40:41]\n" \
    "s_add_u32 m0, s22, 117760\n" \
    "s_nop 0\n" \
    "global_load_lds_dwordx4 v87, s[40:41]\n" \
    "s_waitcnt vmcnt(16)\n" \
    "s_waitcnt lgkmcnt(0)\n" \
    "s_barrier\n" \
    "ds_read_b64_tr_b16 v[232:233], v88\n" \
    "ds_read_b64_tr_b16 v[234:235], v88 offset:2048\n" \
    "ds_read_b64_tr_b16 v[236:237], v88 offset:8192\n" \
    "ds_read_b64_tr_b16 v[238:239], v88 offset:10240\n" \
    "ds_read_b64_tr_b16 v[224:225], v89\n" \
    "ds_read_b64_tr_b16 v[226:227], v89 offset:2048\n" \
    "ds_read_b64_tr_b16 v[228:229], v89 offset:8192\n" \
    "ds_read_b64_tr_b16 v[230:231], v89 offset:10240\n" \
    "ds_read_b64_tr_b16 v[200:201], v88 offset:16384\n" \
    "ds_read_b64_tr_b16 v[202:203], v88 offset:18432\n" \
    "ds_read_b64_tr_b16 v[204:205], v88 offset:24576\n" \
    "ds_read_b64_tr_b16 v[206:207], v88 offset:26624\n" \
    "ds_read_b64_tr_b16 v[192:193], v89 offset:16384\n" \
    "ds_read_b64_tr_b16 v[194:195], v89 offset:18432\n" \
    "ds_read_b64_tr_b16 v[196:197], v89 offset:24576\n" \
    "ds_read_b64_tr_b16 v[198:199], v89 offset:26624\n" \
    "ds_read_b64_tr_b16 v[168:169], v88 offset:32768\n" \
    "ds_read_b64_tr_b16 v[170:171], v88 offset:34816\n" \
    "ds_read_b64_tr_b16 v[172:173], v88 offset:40960\n" \
    "ds_read_b64_tr_b16 v[174:175], v88 offset:43008\n" \
    "ds_read_b64_tr_b16 v[160:161], v89 offset:32768\n" \
    "ds_read_b64_tr_b16 v[162:163], v89 offset:34816\n" \
    "ds_read_b64_tr_b16 v[164:165], v89 offset:40960\n" \
    "ds_read_b64_tr_b16 v[166:167], v89 offset:43008\n" \
    "ds_read_b64_tr_b16 v[152:153], v88 offset:49152\n" \
    "ds_read_b64_tr_b16 v[154:155], v88 offset:51200\n" \
    "ds_read_b64_tr_b16 v[156:157], v88 offset:57344\n" \
    "ds_read_b64_tr_b16 v[158:159], v88 offset:59392\n" \
    "ds_read_b64_tr_b16 v[144:145], v89 offset:49152\n" \
    "ds_read_b64_tr_b16 v[146:147], v89 offset:51200\n" \
    "ds_read_b64_tr_b16 v[148:149], v89 offset:57344\n" \
    "ds_read_b64_tr_b16 v[150:151], v89 offset:59392\n" \
    "s_waitcnt lgkmcnt(0)\n" \
    "s_barrier\n" \
    "s_add_u32 s40, s12, 3670016\n" \
    "s_addc_u32 s41, s13, 0\n" \
    "s_add_u32 m0, s22, 0\n" \
    "s_nop 0\n" \
    "global_load_lds_dwordx4 v84, s[40:41]\n" \
    "s_add_u32 m0, s22, 1024\n" \
    "s_nop 0\n" \
    "global_load_lds_dwordx4 v85, s[40:41]\n" \
    "s_add_u32 m0, s22, 2048\n" \
    "s_nop 0\n" \
    "global_load_lds_dwordx4 v86, s[40:41]\n" \
    "s_add_u32 m0, s22, 3072\n" \
    "s_nop 0\n" \
    "global_load_lds_dwordx4 v87, s[40:41]\n" \
    "s_add_u32 s40, s12, 4194304\n" \
    "s_addc_u32 s41, s13, 0\n" \
    "s_add_u32 m0, s22, 16384\n" \
    "s_nop 0\n" \
    "global_load_lds_dwordx4 v84, s[40:41]\n" \
    "s_add_u32 m0, s22, 17408\n" \
    "s_nop 0\n" \
    "global_load_lds_dwordx4 v85, s[40:41]\n" \
    "s_add_u32 m0, s22, 18432\n" \
    "s_nop 0\n" \
    "global_load_lds_dwordx4 v86, s[40:41]\n" \
    "s_add_u32 m0, s22, 19456\n" \
    "s_nop 0\n" \
    "global_load_lds_dwordx4 v87, s[40:41]\n" \
    "s_add_u32 s40, s12, 4718592\n" \
    "s_addc_u32 s41, s13, 0\n" \
    "s_add_u32 m0, s22, 32768\n" \
    "s_nop 0\n" \
    "global_load_lds_dwordx4 v84, s[40:41]\n" \
    "s_add_u32 m0, s22, 33792\n" \
    "s_nop 0\n" \
    "global_load_lds_dwordx4 v85, s[40:41]\n" \
    "s_add_u32 m0, s22, 34816\n" \
    "s_nop 0\n" \
    "global_load_lds_dwordx4 v86, s[40:41]\n" \
    "s_add_u32 m0, s22, 35840\n" \
    "s_nop 0\n" \
    "global_load_lds_dwordx4 v87, s[40:41]\n" \
    "s_waitcnt vmcnt(12)\n" \
    "s_waitcnt lgkmcnt(0)\n" \
    "s_barrier\n" \
    "ds_read_b64_tr_b16 a[32:33], v92\n" \
    "ds_read_b64_tr_b16 a[34:35], v92 offset:2048\n" \
    "ds_read_b64_tr_b16 a[36:37], v92 offset:8192\n" \
    "ds_read_b64_tr_b16 a[38:39], v92 offset:10240\n" \
    "ds_read_b64_tr_b16 a[40:41], v93\n" \
    "ds_read_b64_tr_b16 a[42:43], v93 offset:2048\n" \
    "ds_read_b64_tr_b16 a[44:45], v93 offset:8192\n" \
    "ds_read_b64_tr_b16 a[46:47], v93 offset:10240\n" \
    "ds_read_b64_tr_b16 a[80:81], v92 offset:16384\n" \
    "ds_read_b64_tr_b16 a[82:83], v92 offset:18432\n" \
    "ds_read_b64_tr_b16 a[84:85], v92 offset:24576\n" \
    "ds_read_b64_tr_b16 a[86:87], v92 offset:26624\n" \
    "ds_read_b64_tr_b16 a[88:89], v93 offset:16384\n" \
    "ds_read_b64_tr_b16 a[90:91], v93 offset:18432\n" \
    "ds_read_b64_tr_b16 a[92:93], v93 offset:24576\n" \
    "ds_read_b64_tr_b16 a[94:95], v93 offset:26624\n" \
    "ds_read_b64_tr_b16 a[144:145], v92 offset:32768\n" \
    "ds_read_b64_tr_b16 a[146:147], v92 offset:34816\n" \
    "ds_read_b64_tr_b16 a[148:149], v92 offset:40960\n" \
    "ds_read_b64_tr_b16 a[150:151], v92 offset:43008\n" \
    "ds_read_b64_tr_b16 a[152:153], v93 offset:32768\n" \
    "ds_read_b64_tr_b16 a[154:155], v93 offset:34816\n" \
    "ds_read_b64_tr_b16 a[156:157], v93 offset:40960\n" \
    "ds_read_b64_tr_b16 a[158:159], v93 offset:43008\n" \
    "ds_read_b64_tr_b16 a[176:177], v92 offset:49152\n" \
    "ds_read_b64_tr_b16 a[178:179], v92 offset:51200\n" \
    "ds_read_b64_tr_b16 a[180:181], v92 offset:57344\n" \
    "ds_read_b64_tr_b16 a[182:183], v92 offset:59392\n" \
    "ds_read_b64_tr_b16 a[184:185], v93 offset:49152\n" \
    "ds_read_b64_tr_b16 a[186:187], v93 offset:51200\n" \
    "ds_read_b64_tr_b16 a[188:189], v93 offset:57344\n" \
    "ds_read_b64_tr_b16 a[190:191], v93 offset:59392\n" \
    "s_waitcnt lgkmcnt(0)\n" \
    "s_barrier\n" \
    "ds_write_b128 v11, v[72:75]\n" \
    "ds_write_b128 v11, v[76:79] offset:1024\n" \
    "ds_write_b128 v11, v[80:83] offset:2048\n" \
    "s_add_u32 m0, s23, 65536\n" \
    "s_nop 0\n" \
    "global_load_lds_dwordx4 v16, s[18:19]\n" \
    "s_add_u32 m0, s23, 66560\n" \
    "s_nop 0\n" \
    "global_load_lds_dwordx4 v17, s[18:19]\n" \
    "s_add_u32 m0, s23, 67584\n" \
    "s_nop 0\n" \
    "global_load_lds_dwordx4 v18, s[18:19]\n" \
    "s_add_u32 m0, s23, 68608\n" \
    "s_nop 0\n" \
    "global_load_lds_dwordx4 v19, s[18:19]\n" \
    "s_add_u32 m0, s23, 69632\n" \
    "s_nop 0\n" \
    "global_load_lds_dwordx4 v20, s[18:19]\n" \
    "s_add_u32 m0, s23, 70656\n" \
    "s_nop 0\n" \
    "global_load_lds_dwordx4 v21, s[18:19]\n" \
    "s_add_u32 m0, s23, 71680\n" \
    "s_nop 0\n" \
    "global_load_lds_dwordx4 v22, s[18:19]\n" \
    "s_add_u32 m0, s23, 72704\n" \
    "s_nop 0\n" \
    "global_load_lds_dwordx4 v23, s[18:19]\n" \
    "s_add_u32 m0, s23, 73728\n" \
    "s_nop 0\n" \
    "global_load_lds_dwordx4 v24, s[18:19]\n" \
    "s_add_u32 m0, s23, 74752\n" \
    "s_nop 0\n" \
    "global_load_lds_dwordx4 v25, s[18:19]\n" \
    "s_waitcnt vmcnt(10)\n" \
    "s_waitcnt lgkmcnt(0)\n" \
    "s_barrier\n" \
    "ds_read_b64_tr_b16 v[248:249], v88\n" \
    "ds_read_b64_tr_b16 v[250:251], v88 offset:2048\n" \
    "ds_read_b64_tr_b16 v[252:253], v88 offset:8192\n" \
    "ds_read_b64_tr_b16 v[254:255], v88 offset:10240\n" \
    "ds_read_b64_tr_b16 v[240:241], v89\n" \
    "ds_read_b64_tr_b16 v[242:243], v89 offset:2048\n" \
    "ds_read_b64_tr_b16 v[244:245], v89 offset:8192\n" \
    "ds_read_b64_tr_b16 v[246:247], v89 offset:10240\n" \
    "ds_read_b64_tr_b16 v[216:217], v88 offset:16384\n" \
    "ds_read_b64_tr_b16 v[218:219], v88 offset:18432\n" \
    "ds_read_b64_tr_b16 v[220:221], v88 offset:24576\n" \
    "ds_read_b64_tr_b16 v[222:223], v88 offset:26624\n" \
    "ds_read_b64_tr_b16 v[208:209], v89 offset:16384\n" \
    "ds_read_b64_tr_b16 v[210:211], v89 offset:18432\n" \
    "ds_read_b64_tr_b16 v[212:213], v89 offset:24576\n" \
    "ds_read_b64_tr_b16 v[214:215], v89 offset:26624\n" \
    "ds_read_b64_tr_b16 v[184:185], v88 offset:32768\n" \
    "ds_read_b64_tr_b16 v[186:187], v88 offset:34816\n" \
    "ds_read_b64_tr_b16 v[188:189], v88 offset:40960\n" \
    "ds_read_b64_tr_b16 v[190:191], v88 offset:43008\n" \
    "ds_read_b64_tr_b16 v[176:177], v89 offset:32768\n" \
    "ds_read_b64_tr_b16 v[178:179], v89 offset:34816\n" \
    "ds_read_b64_tr_b16 v[180:181], v89 offset:40960\n" \
    "ds_read_b64_tr_b16 v[182:183], v89 offset:43008\n" \
    "s_waitcnt lgkmcnt(0)\n" \
    "s_barrier\n" \
    "s_add_u32 s40, s12, 0\n" \
    "s_addc_u32 s41, s13, 0\n" \
    "s_add_u32 m0, s22, 0\n" \
    "s_nop 0\n" \
    "global_load_lds_dwordx4 v84, s[40:41]\n" \
    "s_add_u32 m0, s22, 1024\n" \
    "s_nop 0\n" \
    "global_load_lds_dwordx4 v85, s[40:41]\n" \
    "s_add_u32 m0, s22, 2048\n" \
    "s_nop 0\n" \
    "global_load_lds_dwordx4 v86, s[40:41]\n" \
    "s_add_u32 m0, s22, 3072\n" \
    "s_nop 0\n" \
    "global_load_lds_dwordx4 v87, s[40:41]\n" \
    "s_add_u32 s40, s14, 1048576\n" \
    "s_addc_u32 s41, s15, 0\n" \
    "s_add_u32 m0, s22, 16384\n" \
    "s_nop 0\n" \
    "global_load_lds_dwordx4 v84, s[40:41]\n" \
    "s_add_u32 m0, s22, 17408\n" \
    "s_nop 0\n" \
    "global_load_lds_dwordx4 v85, s[40:41]\n" \
    "s_add_u32 m0, s22, 18432\n" \
    "s_nop 0\n" \
    "global_load_lds_dwordx4 v86, s[40:41]\n" \
    "s_add_u32 m0, s22, 19456\n" \
    "s_nop 0\n" \
    "global_load_lds_dwordx4 v87, s[40:41]\n" \
    "s_add_u32 s40, s12, 1572864\n" \
    "s_addc_u32 s41, s13, 0\n" \
    "s_add_u32 m0, s22, 32768\n" \
    "s_nop 0\n" \
    "global_load_lds_dwordx4 v84, s[40:41]\n" \
    "s_add_u32 m0, s22, 33792\n" \
    "s_nop 0\n" \
    "global_load_lds_dwordx4 v85, s[40:41]\n" \
    "s_add_u32 m0, s22, 34816\n" \
    "s_nop 0\n" \
    "global_load_lds_dwordx4 v86, s[40:41]\n" \
    "s_add_u32 m0, s22, 35840\n" \
    "s_nop 0\n" \
    "global_load_lds_dwordx4 v87, s[40:41]\n" \
    "s_add_u32 s40, s14, 2097152\n" \
    "s_addc_u32 s41, s15, 0\n" \
    "s_add_u32 m0, s22, 49152\n" \
    "s_nop 0\n" \
    "global_load_lds_dwordx4 v84, s[40:41]\n" \
    "s_add_u32 m0, s22, 50176\n" \
    "s_nop 0\n" \
    "global_load_lds_dwordx4 v85, s[40:41]\n" \
    "s_add_u32 m0, s22, 51200\n" \
    "s_nop 0\n" \
    "global_load_lds_dwordx4 v86, s[40:41]\n" \
    "s_add_u32 m0, s22, 52224\n" \
    "s_nop 0\n" \
    "global_load_lds_dwordx4 v87, s[40:41]\n" \
    "s_waitcnt vmcnt(16)\n" \
    "s_waitcnt lgkmcnt(0)\n" \
    "s_barrier\n" \
    "ds_read_b64_tr_b16 v[136:137], v90\n" \
    "ds_read_b64_tr_b16 v[138:139], v90 offset:5120\n" \
    "ds_read_b64_tr_b16 v[140:141], v90 offset:10240\n" \
    "ds_read_b64_tr_b16 v[142:143], v90 offset:15360\n" \
    "ds_read_b64_tr_b16 v[128:129], v90 offset:20480\n" \
    "ds_read_b64_tr_b16 v[130:131], v90 offset:25600\n" \
    "ds_read_b64_tr_b16 v[132:133], v90 offset:30720\n" \
    "ds_read_b64_tr_b16 v[134:135], v90 offset:35840\n" \
    "ds_read_b64_tr_b16 v[120:121], v90 offset:32\n" \
    "ds_read_b64_tr_b16 v[122:123], v90 offset:5152\n" \
    "ds_read_b64_tr_b16 v[124:125], v90 offset:10272\n" \
    "ds_read_b64_tr_b16 v[126:127], v90 offset:15392\n" \
    "ds_read_b64_tr_b16 v[112:113], v90 offset:20512\n" \
    "ds_read_b64_tr_b16 v[114:115], v90 offset:25632\n" \
    "ds_read_b64_tr_b16 v[116:117], v90 offset:30752\n" \
    "ds_read_b64_tr_b16 v[118:119], v90 offset:35872\n" \
    "ds_read_b64_tr_b16 v[104:105], v90 offset:64\n" \
    "ds_read_b64_tr_b16 v[106:107], v90 offset:5184\n" \
    "ds_read_b64_tr_b16 v[108:109], v90 offset:10304\n" \
    "ds_read_b64_tr_b16 v[110:111], v90 offset:15424\n" \
    "ds_read_b64_tr_b16 v[96:97], v90 offset:20544\n" \
    "ds_read_b64_tr_b16 v[98:99], v90 offset:25664\n" \
    "ds_read_b64_tr_b16 v[100:101], v90 offset:30784\n" \
    "ds_read_b64_tr_b16 v[102:103], v90 offset:35904\n" \
    "s_waitcnt lgkmcnt(0)\n" \
    "s_barrier\n" \
    "s_add_u32 s40, s12, 3145728\n" \
    "s_addc_u32 s41, s13, 0\n" \
    "s_add_u32 m0, s22, 65536\n" \
    "s_nop 0\n" \
    "global_load_lds_dwordx4 v84, s[40:41]\n" \
    "s_add_u32 m0, s22, 66560\n" \
    "s_nop 0\n" \
    "global_load_lds_dwordx4 v85, s[40:41]\n" \
    "s_add_u32 m0, s22, 67584\n" \
    "s_nop 0\n" \
    "global_load_lds_dwordx4 v86, s[40:41]\n" \
    "s_add_u32 m0, s22, 68608\n" \
    "s_nop 0\n" \
    "global_load_lds_dwordx4 v87, s[40:41]\n" \
    "s_add_u32 s40, s14, 3670016\n" \
    "s_addc_u32 s41, s15, 0\n" \
    "s_add_u32 m0, s22, 81920\n" \
    "s_nop 0\n" \
    "global_load_lds_dwordx4 v84, s[40:41]\n" \
    "s_add_u32 m0, s22, 82944\n" \
    "s_nop 0\n" \
    "global_load_lds_dwordx4 v85, s[40:41]\n" \
    "s_add_u32 m0, s22, 83968\n" \
    "s_nop 0\n" \
    "global_load_lds_dwordx4 v86, s[40:41]\n" \
    "s_add_u32 m0, s22, 84992\n" \
    "s_nop 0\n" \
    "global_load_lds_dwordx4 v87, s[40:41]\n" \
    "s_add_u32 s40, s14, 4194304\n" \
    "s_addc_u32 s41, s15, 0\n" \
    "s_add_u32 m0, s22, 98304\n" \
    "s_nop 0\n" \
    "global_load_lds_dwordx4 v84, s[40:41]\n" \
    "s_add_u32 m0, s22, 99328\n" \
    "s_nop 0\n" \
    "global_load_lds_dwordx4 v85, s[40:41]\n" \
    "s_add_u32 m0, s22, 100352\n" \
    "s_nop 0\n" \
    "global_load_lds_dwordx4 v86, s[40:41]\n" \
    "s_add_u32 m0, s22, 101376\n" \
    "s_nop 0\n" \
    "global_load_lds_dwordx4 v87, s[40:41]\n" \
    "s_waitcnt vmcnt(12)\n" \
    "s_waitcnt lgkmcnt(0)\n" \
    "s_barrier\n" \
    "ds_read_b64_tr_b16 v[16:17], v88\n" \
    "ds_read_b64_tr_b16 v[18:19], v88 offset:2048\n" \
    "ds_read_b64_tr_b16 v[20:21], v88 offset:8192\n" \
    "ds_read_b64_tr_b16 v[22:23], v88 offset:10240\n" \
    "ds_read_b64_tr_b16 v[24:25], v89\n" \
    "ds_read_b64_tr_b16 v[26:27], v89 offset:2048\n" \
    "ds_read_b64_tr_b16 v[28:29], v89 offset:8192\n" \
    "ds_read_b64_tr_b16 v[30:31], v89 offset:10240\n" \
    "ds_read_b64_tr_b16 v[32:33], v88 offset:16384\n" \
    "ds_read_b64_tr_b16 v[34:35], v88 offset:18432\n" \
    "ds_read_b64_tr_b16 v[36:37], v88 offset:24576\n" \
    "ds_read_b64_tr_b16 v[38:39], v88 offset:26624\n" \
    "ds_read_b64_tr_b16 v[40:41], v89 offset:16384\n" \
    "ds_read_b64_tr_b16 v[42:43], v89 offset:18432\n" \
    "ds_read_b64_tr_b16 v[44:45], v89 offset:24576\n" \
    "ds_read_b64_tr_b16 v[46:47], v89 offset:26624\n" \
    "ds_read_b64_tr_b16 v[48:49], v88 offset:32768\n" \
    "ds_read_b64_tr_b16 v[50:51], v88 offset:34816\n" \
    "ds_read_b64_tr_b16 v[52:53], v88 offset:40960\n" \
    "ds_read_b64_tr_b16 v[54:55], v88 offset:43008\n" \
    "ds_read_b64_tr_b16 v[56:57], v89 offset:32768\n" \
    "ds_read_b64_tr_b16 v[58:59], v89 offset:34816\n" \
    "ds_read_b64_tr_b16 v[60:61], v89 offset:40960\n" \
    "ds_read_b64_tr_b16 v[62:63], v89 offset:43008\n" \
    "ds_read_b64_tr_b16 v[64:65], v88 offset:49152\n" \
    "ds_read_b64_tr_b16 v[66:67], v88 offset:51200\n" \
    "ds_read_b64_tr_b16 v[68:69], v88 offset:57344\n" \
    "ds_read_b64_tr_b16 v[70:71], v88 offset:59392\n" \
    "ds_read_b64_tr_b16 v[72:73], v89 offset:49152\n" \
    "ds_read_b64_tr_b16 v[74:75], v89 offset:51200\n" \
    "ds_read_b64_tr_b16 v[76:77], v89 offset:57344\n" \
    "ds_read_b64_tr_b16 v[78:79], v89 offset:59392\n" \
    "s_waitcnt lgkmcnt(0)\n" \
    "s_barrier\n" \
    "ds_write_b128 v2, v[16:19]\n" \
    "ds_write_b128 v2, v[20:23] offset:1024\n" \
    "ds_write_b128 v2, v[24:27] offset:2048\n" \
    "ds_write_b128 v2, v[28:31] offset:3072\n" \
    "ds_write_b128 v2, v[32:35] offset:16384\n" \
    "ds_write_b128 v2, v[36:39] offset:17408\n" \
    "ds_write_b128 v2, v[40:43] offset:18432\n" \
    "ds_write_b128 v2, v[44:47] offset:19456\n" \
    "ds_write_b128 v2, v[48:51] offset:32768\n" \
    "ds_write_b128 v2, v[52:55] offset:33792\n" \
    "ds_write_b128 v2, v[56:59] offset:34816\n" \
    "ds_write_b128 v2, v[60:63] offset:35840\n" \
    "ds_write_b128 v2, v[64:67] offset:49152\n" \
    "ds_write_b128 v2, v[68:71] offset:50176\n" \
    "ds_write_b128 v2, v[72:75] offset:51200\n" \
    "ds_write_b128 v2, v[76:79] offset:52224\n" \
    "s_waitcnt lgkmcnt(0)\n" \
    "s_cmp_gt_u32 s38, s42\n" \
    "s_cbranch_scc1 KVOKs_3_%=\n" \
    "KVPs_3_%=:\n" \
    "buffer_load_dword v8, off, s[8:11], s24 sc1\n" \
    "s_waitcnt vmcnt(0)\n" \
    "v_readfirstlane_b32 s38, v8\n" \
    "s_nop 0\n" \
    "s_cmp_gt_u32 s38, s42\n" \
    "s_cbranch_scc1 KVOKs_3_%=\n" \
    "s_add_u32 s39, s39, 1\n" \
    "s_cmp_lt_u32 s39, 1048576\n" \
    "s_sleep 1\n" \
    "s_cbranch_scc1 KVPs_3_%=\n" \
    "s_branch FAIL_3_%=\n" \
    "KVOKs_3_%=:\n" \
    "s_mov_b32 s40, 0xa00000\n" \
    "s_mov_b32 m0, s43\n" \
    "s_nop 0\n" \
    "buffer_load_dwordx4 v15, s[8:11], s40 offen sc1 lds\n" \
    "buffer_load_dwordx4 v15, s[8:11], s40 offen offset:1024 sc1 lds\n" \
    "buffer_load_dwordx4 v15, s[8:11], s40 offen offset:2048 sc1 lds\n" \
    "buffer_load_dwordx4 v15, s[8:11], s40 offen offset:3072 sc1 lds\n" \
    "s_mov_b32 s40, 0x1b00000\n" \
    "s_mov_b32 m0, s37\n" \
    "s_nop 0\n" \
    "buffer_load_dwordx4 v15, s[8:11], s40 offen sc1 lds\n" \
    "buffer_load_dwordx4 v15, s[8:11], s40 offen offset:1024 sc1 lds\n" \
    "buffer_load_dwordx4 v15, s[8:11], s40 offen offset:2048 sc1 lds\n" \
    "buffer_load_dwordx4 v15, s[8:11], s40 offen offset:3072 sc1 lds\n" \
    "s_mov_b32 s40, 0x1b40000\n" \
    "s_xor_b32 s41, s37, 8192\n" \
    "s_mov_b32 m0, s41\n" \
    "s_nop 0\n" \
    "buffer_load_dwordx4 v15, s[8:11], s40 offen sc1 lds\n" \
    "buffer_load_dwordx4 v15, s[8:11], s40 offen offset:1024 sc1 lds\n" \
    "buffer_load_dwordx4 v15, s[8:11], s40 offen offset:2048 sc1 lds\n" \
    "buffer_load_dwordx4 v15, s[8:11], s40 offen offset:3072 sc1 lds\n" \
    "s_waitcnt vmcnt(0)\n" \
    "s_waitcnt lgkmcnt(0)\n" \
    "s_barrier\n" \
    "ds_read_b64_tr_b16 v[16:17], v92\n" \
    "ds_read_b64_tr_b16 v[18:19], v92 offset:2048\n" \
    "ds_read_b64_tr_b16 v[20:21], v92 offset:8192\n" \
    "ds_read_b64_tr_b16 v[22:23], v92 offset:10240\n" \
    "ds_read_b64_tr_b16 v[24:25], v93\n" \
    "ds_read_b64_tr_b16 v[26:27], v93 offset:2048\n" \
    "ds_read_b64_tr_b16 v[28:29], v93 offset:8192\n" \
    "ds_read_b64_tr_b16 v[30:31], v93 offset:10240\n" \
    "ds_read_b64_tr_b16 v[32:33], v92 offset:16384\n" \
    "ds_read_b64_tr_b16 v[34:35], v92 offset:18432\n" \
    "ds_read_b64_tr_b16 v[36:37], v92 offset:24576\n" \
    "ds_read_b64_tr_b16 v[38:39], v92 offset:26624\n" \
    "ds_read_b64_tr_b16 v[40:41], v93 offset:16384\n" \
    "ds_read_b64_tr_b16 v[42:43], v93 offset:18432\n" \
    "ds_read_b64_tr_b16 v[44:45], v93 offset:24576\n" \
    "ds_read_b64_tr_b16 v[46:47], v93 offset:26624\n" \
    "ds_read_b64_tr_b16 v[48:49], v92 offset:32768\n" \
    "ds_read_b64_tr_b16 v[50:51], v92 offset:34816\n" \
    "ds_read_b64_tr_b16 v[52:53], v92 offset:40960\n" \
    "ds_read_b64_tr_b16 v[54:55], v92 offset:43008\n" \
    "ds_read_b64_tr_b16 v[56:57], v93 offset:32768\n" \
    "ds_read_b64_tr_b16 v[58:59], v93 offset:34816\n" \
    "ds_read_b64_tr_b16 v[60:61], v93 offset:40960\n" \
    "ds_read_b64_tr_b16 v[62:63], v93 offset:43008\n" \
    "s_waitcnt lgkmcnt(0)\n" \
    "s_barrier\n" \
    "ds_write_b128 v3, v[16:19]\n" \
    "ds_write_b128 v3, v[20:23] offset:1024\n" \
    "ds_write_b128 v3, v[24:27] offset:2048\n" \
    "ds_write_b128 v3, v[28:31] offset:3072\n" \
    "ds_write_b128 v3, v[32:35] offset:16384\n" \
    "ds_write_b128 v3, v[36:39] offset:17408\n" \
    "ds_write_b128 v3, v[40:43] offset:18432\n" \
    "ds_write_b128 v3, v[44:47] offset:19456\n" \
    "ds_write_b128 v3, v[48:51] offset:32768\n" \
    "ds_write_b128 v3, v[52:55] offset:33792\n" \
    "ds_write_b128 v3, v[56:59] offset:34816\n" \
    "ds_write_b128 v3, v[60:63] offset:35840\n" \
    "s_waitcnt lgkmcnt(0)\n" \
    "s_barrier\n" \
    "ds_read_b128 v[16:19], v12\n" \
    "ds_read_b128 v[20:23], v12 offset:4096\n" \
    "ds_read_b128 v[24:27], v12 offset:1024\n" \
    "ds_read_b128 v[28:31], v12 offset:5120\n" \
    "ds_read_b128 v[32:35], v12 offset:2048\n" \
    "ds_read_b128 v[36:39], v12 offset:6144\n" \
    "ds_read_b128 v[40:43], v12 offset:3072\n" \
    "ds_read_b128 v[44:47], v12 offset:7168\n" \
    "v_xor_b32 v12, 0x2000, v12\n" \
    "s_waitcnt lgkmcnt(7)\n" \
    "v_mfma_f32_16x16x32_bf16 v[48:51], v[16:19], v[136:139], 0\n" \
    "ds_read_b128 v[76:79], v1 offset:40960\n" \
    "s_waitcnt lgkmcnt(7)\n" \
    "v_mfma_f32_16x16x32_bf16 v[52:55], v[20:23], v[136:139], 0\n" \
    "ds_read_b128 v[80:83], v1 offset:40960\n" \
    "v_mfma_f32_16x16x32_bf16 v[56:59], v[16:19], v[120:123], 0\n" \
    "v_mfma_f32_16x16x32_bf16 v[60:63], v[20:23], v[120:123], 0\n" \
    "v_mfma_f32_16x16x32_bf16 v[64:67], v[16:19], v[104:107], 0\n" \
    "v_mfma_f32_16x16x32_bf16 v[68:71], v[20:23], v[104:107], 0\n" \
    "s_waitcnt lgkmcnt(7)\n" \
    "v_mfma_f32_16x16x32_bf16 v[48:51], v[24:27], v[140:143], v[48:51]\n" \
    "s_waitcnt lgkmcnt(6)\n" \
    "v_mfma_f32_16x16x32_bf16 v[52:55], v[28:31], v[140:143], v[52:55]\n" \
    "v_mfma_f32_16x16x32_bf16 v[56:59], v[24:27], v[124:127], v[56:59]\n" \
    "v_mfma_f32_16x16x32_bf16 v[60:63], v[28:31], v[124:127], v[60:63]\n" \
    "v_mfma_f32_16x16x32_bf16 v[64:67], v[24:27], v[108:111], v[64:67]\n" \
    "v_mfma_f32_16x16x32_bf16 v[68:71], v[28:31], v[108:111], v[68:71]\n" \
    "s_waitcnt lgkmcnt(5)\n" \
    "v_mfma_f32_16x16x32_bf16 v[48:51], v[32:35], v[128:131], v[48:51]\n" \
    "ds_read_b128 v[16:19], v1 offset:40960\n" \
    "s_waitcnt lgkmcnt(5)\n" \
    "v_mfma_f32_16x16x32_bf16 v[52:55], v[36:39], v[128:131], v[52:55]\n" \
    "ds_read_b128 v[20:23], v1 offset:40960\n" \
    "v_mfma_f32_16x16x32_bf16 v[56:59], v[32:35], v[112:115], v[56:59]\n" \
    "ds_read_b128 v[24:27], v1 offset:40960\n" \
    "v_mfma_f32_16x16x32_bf16 v[60:63], v[36:39], v[112:115], v[60:63]\n" \
    "ds_read_b128 v[28:31], v1 offset:40960\n" \
    "v_mfma_f32_16x16x32_bf16 v[64:67], v[32:35], v[96:99], v[64:67]\n" \
    "v_mfma_f32_16x16x32_bf16 v[68:71], v[36:39], v[96:99], v[68:71]\n" \
    "s_waitcnt lgkmcnt(7)\n" \
    "v_mfma_f32_16x16x32_bf16 v[48:51], v[40:43], v[132:135], v[48:51]\n" \
    "s_waitcnt lgkmcnt(6)\n" \
    "v_mfma_f32_16x16x32_bf16 v[52:55], v[44:47], v[132:135], v[52:55]\n" \
    "v_mfma_f32_16x16x32_bf16 v[56:59], v[40:43], v[116:119], v[56:59]\n" \
    "v_mfma_f32_16x16x32_bf16 v[60:63], v[44:47], v[116:119], v[60:63]\n" \
    "v_mfma_f32_16x16x32_bf16 v[64:67], v[40:43], v[100:103], v[64:67]\n" \
    "v_mfma_f32_16x16x32_bf16 v[68:71], v[44:47], v[100:103], v[68:71]\n" \
    "v_mov_b32 v8, v8\n" \
    "v_mov_b32 v8, v8\n" \
    "v_cvt_pk_bf16_f32 v84, v48, v49\n" \
    "v_cvt_pk_bf16_f32 v85, v50, v51\n" \
    "v_cvt_pk_bf16_f32 v86, v52, v53\n" \
    "v_cvt_pk_bf16_f32 v87, v54, v55\n" \
    "v_cvt_pk_bf16_f32 v88, v56, v57\n" \
    "v_cvt_pk_bf16_f32 v89, v58, v59\n" \
    "v_cvt_pk_bf16_f32 v90, v60, v61\n" \
    "v_cvt_pk_bf16_f32 v91, v62, v63\n" \
    "v_cvt_pk_bf16_f32 v92, v64, v65\n" \
    "v_cvt_pk_bf16_f32 v93, v66, v67\n" \
    "v_cvt_pk_bf16_f32 v94, v68, v69\n" \
    "v_cvt_pk_bf16_f32 v95, v70, v71\n" \
    "ds_read_b128 v[48:51], v2\n" \
    "ds_read_b128 v[52:55], v2 offset:1024\n" \
    "ds_read_b128 v[56:59], v2 offset:2048\n" \
    "ds_read_b128 v[60:63], v2 offset:3072\n" \
    "ds_read_b128 v[64:67], v2 offset:16384\n" \
    "ds_read_b128 v[68:71], v2 offset:17408\n" \
    ".p2align 6\n" \
    "LOOP_3_%=:\n" \
    "s_waitcnt lgkmcnt(0)\n" \
    "s_barrier\n" \
    "ds_read_b128 v[32:35], v1\n" \
    "ds_read_b128 v[36:39], v1 offset:1024\n" \
    "ds_read_b128 v[40:43], v1 offset:2048\n" \
    "ds_read_b128 v[44:47], v1 offset:3072\n" \
    "s_bitcmp1_b32 s26, 0\n" \
    "s_cbranch_scc0 NOFLAG_3_%=\n" \
    "v_mov_b32 v72, s28\n" \
    "buffer_store_dword v72, off, s[8:11], s25 sc1\n" \
    "NOFLAG_3_%=:\n" \
    "s_waitcnt lgkmcnt(3)\n" \
    "v_smfmac_f32_16x16x64_bf16 v[16:19], v[32:35], v[48:55], v0\n" \
    "ds_read_b128 v[48:51], v2 offset:18432\n" \
    "ds_read_b128 v[52:55], v2 offset:19456\n" \
    "v_smfmac_f32_16x16x64_bf16 v[24:27], v[32:35], a[0:7], v0\n" \
    "s_mov_b32 m0, s37\n" \
    "v_smfmac_f32_16x16x64_bf16 v[28:31], v[32:35], a[8:15], v0\n" \
    "buffer_load_dwordx4 v15, s[8:11], s30 offen sc1 lds\n" \
    "v_smfmac_f32_16x16x64_bf16 v[20:23], v[32:35], v[56:63], v0\n" \
    "ds_read_b128 v[32:35], v1 offset:4096\n" \
    "ds_read_b128 v[56:59], v2 offset:32768\n" \
    "ds_read_b128 v[60:63], v2 offset:33792\n" \
    "s_waitcnt lgkmcnt(7)\n" \
    "v_smfmac_f32_16x16x64_bf16 v[16:19], v[36:39], a[32:39], v0\n" \
    "v_smfmac_f32_16x16x64_bf16 v[24:27], v[36:39], a[48:55], v0\n" \
    "v_smfmac_f32_16x16x64_bf16 v[28:31], v[36:39], a[56:63], v0\n" \
    "buffer_load_dwordx4 v15, s[8:11], s30 offen offset:1024 sc1 lds\n" \
    "v_smfmac_f32_16x16x64_bf16 v[20:23], v[36:39], a[40:47], v0\n" \
    "ds_read_b128 v[36:39], v1 offset:5120\n" \
    "s_waitcnt lgkmcnt(7)\n" \
    "v_smfmac_f32_16x16x64_bf16 v[24:27], v[40:43], v[64:71], v0\n" \
    "ds_read_b128 v[64:67], v2 offset:34816\n" \
    "ds_read_b128 v[68:71], v2 offset:35840\n" \
    "v_smfmac_f32_16x16x64_bf16 v[16:19], v[40:43], a[80:87], v0\n" \
    "v_smfmac_f32_16x16x64_bf16 v[20:23], v[40:43], a[88:95], v0\n" \
    "buffer_load_dwordx4 v15, s[8:11], s30 offen offset:2048 sc1 lds\n" \
    "s_waitcnt lgkmcnt(6)\n" \
    "v_smfmac_f32_16x16x64_bf16 v[28:31], v[40:43], v[48:55], v0\n" \
    "ds_read_b128 v[40:43], v1 offset:6144\n" \
    "ds_read_b128 v[48:51], v2 offset:49152\n" \
    "ds_read_b128 v[52:55], v2 offset:50176\n" \
    "s_waitcnt lgkmcnt(6)\n" \
    "v_smfmac_f32_16x16x64_bf16 v[16:19], v[44:47], v[56:63], v0\n" \
    "ds_read_b128 v[56:59], v2 offset:51200\n" \
    "ds_read_b128 v[60:63], v2 offset:52224\n" \
    "v_smfmac_f32_16x16x64_bf16 v[24:27], v[44:47], a[112:119], v0\n" \
    "v_smfmac_f32_16x16x64_bf16 v[28:31], v[44:47], a[120:127], v0\n" \
    "buffer_load_dwordx4 v15, s[8:11], s30 offen offset:3072 sc1 lds\n" \
    "s_waitcnt lgkmcnt(5)\n" \
    "v_smfmac_f32_16x16x64_bf16 v[20:23], v[44:47], v[64:71], v0\n" \
    "ds_read_b128 v[44:47], v1 offset:7168\n" \
    "ds_read_b128 v[64:67], v3\n" \
    "ds_read_b128 v[68:71], v3 offset:1024\n" \
    "s_waitcnt lgkmcnt(5)\n" \
    "v_smfmac_f32_16x16x64_bf16 v[24:27], v[32:35], v[48:55], v0\n" \
    "ds_read_b128 v[48:51], v3 offset:2048\n" \
    "ds_read_b128 v[52:55], v3 offset:3072\n" \
    "v_smfmac_f32_16x16x64_bf16 v[16:19], v[32:35], a[144:151], v0\n" \
    "s_add_u32 s30, s30, 262144\n" \
    "s_xor_b32 s37, s37, 8192\n" \
    "s_mov_b32 m0, s43\n" \
    "v_smfmac_f32_16x16x64_bf16 v[20:23], v[32:35], a[152:159], v0\n" \
    "s_waitcnt lgkmcnt(5)\n" \
    "v_smfmac_f32_16x16x64_bf16 v[28:31], v[32:35], v[56:63], v0\n" \
    "ds_read_b128 v[32:35], v1 offset:8192\n" \
    "ds_read_b128 v[56:59], v3 offset:16384\n" \
    "ds_read_b128 v[60:63], v3 offset:17408\n" \
    "v_smfmac_f32_16x16x64_bf16 v[16:19], v[36:39], a[176:183], v0\n" \
    "v_smfmac_f32_16x16x64_bf16 v[24:27], v[36:39], a[192:199], v0\n" \
    "ds_read_b128 v[72:75], v1 offset:13312\n" \
    "v_smfmac_f32_16x16x64_bf16 v[28:31], v[36:39], a[200:207], v0\n" \
    "v_smfmac_f32_16x16x64_bf16 v[20:23], v[36:39], a[184:191], v0\n" \
    "ds_read_b128 v[36:39], v1 offset:9216\n" \
    "s_waitcnt lgkmcnt(7)\n" \
    "v_smfmac_f32_16x16x64_bf16 v[16:19], v[40:43], v[64:71], v0\n" \
    "ds_read_b128 v[64:67], v3 offset:18432\n" \
    "ds_read_b128 v[68:71], v3 offset:19456\n" \
    "v_smfmac_f32_16x16x64_bf16 v[24:27], v[40:43], a[224:231], v0\n" \
    "v_smfmac_f32_16x16x64_bf16 v[28:31], v[40:43], a[232:239], v0\n" \
    "s_waitcnt lgkmcnt(7)\n" \
    "v_smfmac_f32_16x16x64_bf16 v[20:23], v[40:43], v[48:55], v0\n" \
    "ds_read_b128 v[48:51], v3 offset:32768\n" \
    "ds_read_b128 v[52:55], v3 offset:33792\n" \
    "s_waitcnt lgkmcnt(6)\n" \
    "v_smfmac_f32_16x16x64_bf16 v[24:27], v[44:47], v[56:63], v0\n" \
    "ds_read_b128 v[56:59], v3 offset:34816\n" \
    "ds_read_b128 v[60:63], v3 offset:35840\n" \
    "v_smfmac_f32_16x16x64_bf16 v[16:19], v[44:47], v[248:255], v0\n" \
    "v_smfmac_f32_16x16x64_bf16 v[20:23], v[44:47], v[240:247], v0\n" \
    "s_waitcnt lgkmcnt(4)\n" \
    "v_smfmac_f32_16x16x64_bf16 v[28:31], v[44:47], v[64:71], v0\n" \
    "s_waitcnt lgkmcnt(2)\n" \
    "v_smfmac_f32_16x16x64_bf16 v[24:27], v[32:35], v[48:55], v0\n" \
    "v_smfmac_f32_16x16x64_bf16 v[16:19], v[32:35], v[216:223], v0\n" \
    "ds_read_b128 v[64:67], v1 offset:12288\n" \
    "v_smfmac_f32_16x16x64_bf16 v[16:19], v[36:39], v[184:191], v0\n" \
    "ds_read_b128 v[68:71], v1 offset:16384\n" \
    "ds_read_b128 v[48:51], v1 offset:10240\n" \
    "v_smfmac_f32_16x16x64_bf16 v[24:27], v[36:39], v[168:175], v0\n" \
    "ds_read_b128 v[52:55], v1 offset:14336\n" \
    "s_waitcnt lgkmcnt(4)\n" \
    "v_smfmac_f32_16x16x64_bf16 v[28:31], v[32:35], v[56:63], v0\n" \
    "v_smfmac_f32_16x16x64_bf16 v[20:23], v[32:35], v[208:215], v0\n" \
    "v_smfmac_f32_16x16x64_bf16 v[28:31], v[36:39], v[160:167], v0\n" \
    "ds_read_b128 v[56:59], v1 offset:11264\n" \
    "v_add_f32 v16, v16, v17\n" \
    "v_smfmac_f32_16x16x64_bf16 v[20:23], v[36:39], v[176:183], v0\n" \
    "ds_read_b128 v[60:63], v1 offset:15360\n" \
    "v_add_f32 v24, v24, v25\n" \
    "v_smfmac_f32_16x16x64_bf16 v[76:79], v[40:43], a[240:247], v0\n" \
    "v_add_f32 v18, v18, v19\n" \
    "v_smfmac_f32_16x16x64_bf16 v[80:83], v[40:43], a[248:255], v0\n" \
    "ds_read_b128 v[40:43], v1\n" \
    "v_add_f32 v26, v26, v27\n" \
    "v_smfmac_f32_16x16x64_bf16 v[76:79], v[44:47], v[232:239], v0\n" \
    "v_cvt_pk_bf16_f32 v16, v16, v18\n" \
    "v_add_f32 v28, v28, v29\n" \
    "v_smfmac_f32_16x16x64_bf16 v[80:83], v[44:47], v[224:231], v0\n" \
    "ds_read_b128 v[44:47], v1 offset:1024\n" \
    "v_cvt_pk_bf16_f32 v24, v24, v26\n" \
    "v_add_f32 v20, v20, v21\n" \
    "v_smfmac_f32_16x16x64_bf16 v[76:79], v[32:35], v[200:207], v0\n" \
    "v_add_f32 v30, v30, v31\n" \
    "v_add_f32 v22, v22, v23\n" \
    "v_smfmac_f32_16x16x64_bf16 v[80:83], v[32:35], v[192:199], v0\n" \
    "ds_read_b128 v[32:35], v1 offset:2048\n" \
    "v_cvt_pk_bf16_f32 v28, v28, v30\n" \
    "v_cvt_pk_bf16_f32 v20, v20, v22\n" \
    "v_smfmac_f32_16x16x64_bf16 v[76:79], v[36:39], v[152:159], v0\n" \
    "ds_write2st64_b32 v5, v16, v20 offset0:0 offset1:1\n" \
    "ds_write2st64_b32 v5, v24, v28 offset0:8 offset1:9\n" \
    "v_smfmac_f32_16x16x64_bf16 v[80:83], v[36:39], v[144:151], v0\n" \
    "ds_read_b128 v[36:39], v1 offset:3072\n" \
    "s_waitcnt lgkmcnt(5)\n" \
    "v_smfmac_f32_16x16x64_bf16 v[76:79], v[40:43], a[16:23], v0\n" \
    "v_smfmac_f32_16x16x64_bf16 v[80:83], v[40:43], a[24:31], v0\n" \
    "ds_read_b128 v[40:43], v1 offset:4096\n" \
    "s_waitcnt lgkmcnt(5)\n" \
    "v_smfmac_f32_16x16x64_bf16 v[76:79], v[44:47], a[64:71], v0\n" \
    "s_waitcnt lgkmcnt(2)\n" \
    "s_barrier\n" \
    "ds_read_b64_tr_b16 v[16:17], v4\n" \
    "ds_read_b64_tr_b16 v[18:19], v4 offset:256\n" \
    "v_smfmac_f32_16x16x64_bf16 v[80:83], v[44:47], a[72:79], v0\n" \
    "ds_read_b128 v[44:47], v1 offset:5120\n" \
    "ds_read_b64_tr_b16 v[20:21], v4 offset:512\n" \
    "ds_read_b64_tr_b16 v[22:23], v4 offset:768\n" \
    "v_smfmac_f32_16x16x64_bf16 v[76:79], v[32:35], a[96:103], v0\n" \
    "ds_read_b64_tr_b16 v[24:25], v4 offset:1024\n" \
    "ds_read_b64_tr_b16 v[26:27], v4 offset:1280\n" \
    "v_smfmac_f32_16x16x64_bf16 v[80:83], v[32:35], a[104:111], v0\n" \
    "ds_read_b128 v[32:35], v1 offset:17408\n" \
    "ds_read_b64_tr_b16 v[28:29], v4 offset:1536\n" \
    "ds_read_b64_tr_b16 v[30:31], v4 offset:1792\n" \
    "s_waitcnt lgkmcnt(11)\n" \
    "v_smfmac_f32_16x16x64_bf16 v[76:79], v[36:39], a[128:135], v0\n" \
    "v_smfmac_f32_16x16x64_bf16 v[80:83], v[36:39], a[136:143], v0\n" \
    "s_waitcnt lgkmcnt(10)\n" \
    "v_smfmac_f32_16x16x64_bf16 v[76:79], v[40:43], a[160:167], v0\n" \
    "v_smfmac_f32_16x16x64_bf16 v[80:83], v[40:43], a[168:175], v0\n" \
    "s_waitcnt lgkmcnt(7)\n" \
    "v_smfmac_f32_16x16x64_bf16 v[76:79], v[44:47], a[208:215], v0\n" \
    "v_smfmac_f32_16x16x64_bf16 v[80:83], v[44:47], a[216:223], v0\n" \
    "v_mfma_f32_16x16x32_bf16 v[36:39], v[48:51], v[16:19], 0\n" \
    "v_mfma_f32_16x16x32_bf16 v[40:43], v[52:55], v[16:19], 0\n" \
    "ds_read_b64_tr_b16 v[48:49], v4 offset:2048\n" \
    "ds_read_b64_tr_b16 v[50:51], v4 offset:2304\n" \
    "s_waitcnt lgkmcnt(7)\n" \
    "v_mfma_f32_16x16x32_bf16 v[36:39], v[56:59], v[20:23], v[36:39]\n" \
    "ds_read_b64_tr_b16 v[52:53], v4 offset:2560\n" \
    "ds_read_b64_tr_b16 v[54:55], v4 offset:2816\n" \
    "v_add_f32 v76, v76, v77\n" \
    "v_mfma_f32_16x16x32_bf16 v[40:43], v[60:63], v[20:23], v[40:43]\n" \
    "ds_read_b64_tr_b16 v[56:57], v4 offset:3072\n" \
    "ds_read_b64_tr_b16 v[58:59], v4 offset:3328\n" \
    "v_add_f32 v80, v80, v81\n" \
    "v_add_f32 v78, v78, v79\n" \
    "s_waitcnt lgkmcnt(9)\n" \
    "v_mfma_f32_16x16x32_bf16 v[36:39], v[64:67], v[24:27], v[36:39]\n" \
    "ds_read_b64_tr_b16 v[60:61], v4 offset:3584\n" \
    "ds_read_b64_tr_b16 v[62:63], v4 offset:3840\n" \
    "v_add_f32 v82, v82, v83\n" \
    "v_cvt_pk_bf16_f32 v76, v76, v78\n" \
    "v_mfma_f32_16x16x32_bf16 v[40:43], v[68:71], v[24:27], v[40:43]\n" \
    "v_cvt_pk_bf16_f32 v80, v80, v82\n" \
    "ds_write2st64_b32 v5, v76, v80 offset0:16 offset1:17\n" \
    "s_waitcnt lgkmcnt(9)\n" \
    "v_mfma_f32_16x16x32_bf16 v[36:39], v[72:75], v[28:31], v[36:39]\n" \
    "ds_read_b128 v[80:83], v1 offset:40960\n" \
    "ds_read_b128 v[72:75], v1 offset:40960\n" \
    "v_mfma_f32_16x16x32_bf16 v[40:43], v[32:35], v[28:31], v[40:43]\n" \
    "ds_read_b128 v[76:79], v1 offset:40960\n" \
    "s_waitcnt lgkmcnt(10)\n" \
    "v_mfma_f32_16x16x32_bf16 v[44:47], v[48:51], v[16:19], 0\n" \
    "s_waitcnt lgkmcnt(3)\n" \
    "s_barrier\n" \
    "ds_read_b64_tr_b16 v[64:65], v7\n" \
    "ds_read_b64_tr_b16 v[66:67], v7 offset:64\n" \
    "ds_read_b64_tr_b16 v[68:69], v7 offset:1024\n" \
    "ds_read_b64_tr_b16 v[70:71], v7 offset:1088\n" \
    "v_mfma_f32_16x16x32_bf16 v[44:47], v[52:55], v[20:23], v[44:47]\n" \
    "s_cmp_eq_u32 s28, 63\n" \
    "s_cbranch_scc1 LAST_3_%=\n" \
    "ds_read_b128 v[32:35], v13\n" \
    "v_mfma_f32_16x16x32_bf16 v[44:47], v[56:59], v[24:27], v[44:47]\n" \
    "buffer_load_dwordx4 v15, s[8:11], s29 offen sc1 lds\n" \
    "v_max3_f32 v53, v36, v37, v38\n" \
    "v_max3_f32 v53, v53, v39, v40\n" \
    "v_mfma_f32_16x16x32_bf16 v[44:47], v[60:63], v[28:31], v[44:47]\n" \
    "buffer_load_dwordx4 v15, s[8:11], s29 offen offset:1024 sc1 lds\n" \
    "v_max3_f32 v53, v53, v41, v42\n" \
    "v_max_f32 v53, v53, v43\n" \
    "ds_read_b128 v[28:31], v1 offset:41984\n" \
    "ds_read_b64 v[56:57], v1 offset:43008\n" \
    "v_mov_b32 v55, v53\n" \
    "s_waitcnt lgkmcnt(5)\n" \
    "v_smfmac_f32_16x16x64_bf16 v[76:79], v[64:67], v[136:143], v6\n" \
    "buffer_load_dwordx4 v15, s[8:11], s29 offen offset:2048 sc1 lds\n" \
    "v_smfmac_f32_16x16x64_bf16 v[80:83], v[64:67], v[120:127], v6\n" \
    "buffer_load_dwordx4 v15, s[8:11], s29 offen offset:3072 sc1 lds\n" \
    "v_permlane32_swap_b32 v53, v55\n" \
    "v_max_f32 v53, v53, v55\n" \
    "v_max3_f32 v54, v44, v45, v46\n" \
    "v_max3_f32 v54, v54, v47, v53\n" \
    "v_mov_b32 v55, v54\n" \
    "v_smfmac_f32_16x16x64_bf16 v[72:75], v[64:67], v[104:111], v6\n" \
    "s_waitcnt lgkmcnt(3)\n" \
    "v_smfmac_f32_16x16x64_bf16 v[76:79], v[68:71], v[128:135], v6\n" \
    "v_permlane16_swap_b32 v54, v55\n" \
    "v_max_f32 v54, v54, v55\n" \
    "v_mul_f32 v54, s27, v54\n" \
    "s_bitcmp1_b32 s26, 0\n" \
    "s_cbranch_scc0 NOPUB_3_%=\n" \
    "s_waitcnt lgkmcnt(2)\n" \
    "buffer_store_dwordx4 v[32:35], v14, s[8:11], s34 offen sc1\n" \
    "NOPUB_3_%=:\n" \
    "v_fma_f32 v36, v36, s27, -v54\n" \
    "v_fma_f32 v37, v37, s27, -v54\n" \
    "v_fma_f32 v38, v38, s27, -v54\n" \
    "v_fma_f32 v39, v39, s27, -v54\n" \
    "v_exp_f32 v36, v36\n" \
    "v_exp_f32 v37, v37\n" \
    "v_exp_f32 v38, v38\n" \
    "v_exp_f32 v39, v39\n" \
    "v_fma_f32 v40, v40, s27, -v54\n" \
    "v_fma_f32 v41, v41, s27, -v54\n" \
    "v_fma_f32 v42, v42, s27, -v54\n" \
    "v_fma_f32 v43, v43, s27, -v54\n" \
    "v_exp_f32 v40, v40\n" \
    "v_exp_f32 v41, v41\n" \
    "v_exp_f32 v42, v42\n" \
    "v_exp_f32 v43, v43\n" \
    "v_fma_f32 v44, v44, s27, -v54\n" \
    "v_cvt_pk_bf16_f32 v48, v36, v37\n" \
    "v_cvt_pk_bf16_f32 v49, v38, v39\n" \
    "v_cvt_pk_bf16_f32 v50, v40, v41\n" \
    "v_cvt_pk_bf16_f32 v51, v42, v43\n" \
    "v_smfmac_f32_16x16x64_bf16 v[80:83], v[68:71], v[112:119], v6\n" \
    "v_smfmac_f32_16x16x64_bf16 v[72:75], v[68:71], v[96:103], v6\n" \
    "v_fma_f32 v45, v45, s27, -v54\n" \
    "v_fma_f32 v46, v46, s27, -v54\n" \
    "s_waitcnt lgkmcnt(1)\n" \
    "v_mfma_f32_16x16x32_bf16 v[60:63], v[28:31], v[48:51], 0\n" \
    "v_fma_f32 v47, v47, s27, -v54\n" \
    "v_exp_f32 v44, v44\n" \
    "v_mfma_f32_16x16x32_bf16 v[16:19], v[84:87], v[48:51], 0\n" \
    "v_exp_f32 v45, v45\n" \
    "v_exp_f32 v46, v46\n" \
    "v_mfma_f32_16x16x32_bf16 v[20:23], v[88:91], v[48:51], 0\n" \
    "v_exp_f32 v47, v47\n" \
    "v_cvt_pk_bf16_f32 v52, v44, v45\n" \
    "v_mfma_f32_16x16x32_bf16 v[24:27], v[92:95], v[48:51], 0\n" \
    "v_cvt_pk_bf16_f32 v53, v46, v47\n" \
    "v_cvt_pk_bf16_f32 v76, v76, v77\n" \
    "v_cvt_pk_bf16_f32 v77, v78, v79\n" \
    "s_waitcnt lgkmcnt(0)\n" \
    "v_mfma_f32_16x16x16_bf16 v[60:63], v[56:57], v[52:53], v[60:63]\n" \
    "v_cvt_pk_bf16_f32 v80, v80, v81\n" \
    "v_cvt_pk_bf16_f32 v81, v82, v83\n" \
    "v_mfma_f32_16x16x16_bf16 v[16:19], v[76:77], v[52:53], v[16:19]\n" \
    "v_cvt_pk_bf16_f32 v72, v72, v73\n" \
    "v_cvt_pk_bf16_f32 v73, v74, v75\n" \
    "v_mfma_f32_16x16x16_bf16 v[20:23], v[80:81], v[52:53], v[20:23]\n" \
    "v_mov_b32 v8, v8\n" \
    "v_mfma_f32_16x16x16_bf16 v[24:27], v[72:73], v[52:53], v[24:27]\n" \
    "v_rcp_f32 v54, v60\n" \
    "v_mov_b32 v8, v8\n" \
    "v_mov_b32 v8, v8\n" \
    "v_mov_b32 v8, v8\n" \
    "v_mov_b32 v8, v8\n" \
    "v_mov_b32 v8, v8\n" \
    "v_mov_b32 v8, v8\n" \
    "v_mov_b32 v8, v8\n" \
    "v_mov_b32_dpp v16, v24 quad_perm:[0,1,2,3] row_mask:0xf bank_mask:0xc\n" \
    "v_mov_b32_dpp v17, v25 quad_perm:[0,1,2,3] row_mask:0xf bank_mask:0xc\n" \
    "v_mov_b32_dpp v18, v26 quad_perm:[0,1,2,3] row_mask:0xf bank_mask:0xc\n" \
    "v_mov_b32_dpp v19, v27 quad_perm:[0,1,2,3] row_mask:0xf bank_mask:0xc\n" \
    "v_pk_mul_f32 v[16:17], v[16:17], v[54:55] op_sel_hi:[1,0]\n" \
    "v_pk_mul_f32 v[18:19], v[18:19], v[54:55] op_sel_hi:[1,0]\n" \
    "v_pk_mul_f32 v[20:21], v[20:21], v[54:55] op_sel_hi:[1,0]\n" \
    "v_pk_mul_f32 v[22:23], v[22:23], v[54:55] op_sel_hi:[1,0]\n" \
    "v_cvt_pk_bf16_f32 v16, v16, v17\n" \
    "v_cvt_pk_bf16_f32 v17, v18, v19\n" \
    "v_cvt_pk_bf16_f32 v18, v20, v21\n" \
    "v_cvt_pk_bf16_f32 v19, v22, v23\n" \
    "v_mov_b32 v8, v8\n" \
    "v_permlane16_swap_b32 v16, v18\n" \
    "v_permlane16_swap_b32 v17, v19\n" \
    "v_bfi_b32 v16, 1, s35, v16\n" \
    "v_bfi_b32 v18, 1, s35, v18\n" \
    "v_bfi_b32 v17, 1, s35, v17\n" \
    "v_bfi_b32 v19, 1, s35, v19\n" \
    "s_mov_b64 exec, s[44:45]\n" \
    "buffer_store_dwordx4 v[16:19], v9, s[8:11], 0 offen sc1\n" \
    "s_mov_b64 exec, -1\n" \
    "v_mov_b32 v8, v8\n" \
    "ds_read_b128 v[16:19], v12\n" \
    "ds_read_b128 v[20:23], v12 offset:4096\n" \
    "ds_read_b128 v[24:27], v12 offset:1024\n" \
    "ds_read_b128 v[28:31], v12 offset:5120\n" \
    "ds_read_b128 v[32:35], v12 offset:2048\n" \
    "ds_read_b128 v[36:39], v12 offset:6144\n" \
    "ds_read_b128 v[40:43], v12 offset:3072\n" \
    "ds_read_b128 v[44:47], v12 offset:7168\n" \
    "v_xor_b32 v12, 0x2000, v12\n" \
    "s_waitcnt lgkmcnt(7)\n" \
    "v_mfma_f32_16x16x32_bf16 v[48:51], v[16:19], v[136:139], 0\n" \
    "ds_read_b128 v[76:79], v1 offset:40960\n" \
    "s_waitcnt lgkmcnt(7)\n" \
    "v_mfma_f32_16x16x32_bf16 v[52:55], v[20:23], v[136:139], 0\n" \
    "ds_read_b128 v[80:83], v1 offset:40960\n" \
    "v_mfma_f32_16x16x32_bf16 v[56:59], v[16:19], v[120:123], 0\n" \
    "v_mfma_f32_16x16x32_bf16 v[60:63], v[20:23], v[120:123], 0\n" \
    "v_mfma_f32_16x16x32_bf16 v[64:67], v[16:19], v[104:107], 0\n" \
    "v_mfma_f32_16x16x32_bf16 v[68:71], v[20:23], v[104:107], 0\n" \
    "s_waitcnt lgkmcnt(7)\n" \
    "v_mfma_f32_16x16x32_bf16 v[48:51], v[24:27], v[140:143], v[48:51]\n" \
    "s_waitcnt lgkmcnt(6)\n" \
    "v_mfma_f32_16x16x32_bf16 v[52:55], v[28:31], v[140:143], v[52:55]\n" \
    "v_mfma_f32_16x16x32_bf16 v[56:59], v[24:27], v[124:127], v[56:59]\n" \
    "v_mfma_f32_16x16x32_bf16 v[60:63], v[28:31], v[124:127], v[60:63]\n" \
    "v_mfma_f32_16x16x32_bf16 v[64:67], v[24:27], v[108:111], v[64:67]\n" \
    "v_mfma_f32_16x16x32_bf16 v[68:71], v[28:31], v[108:111], v[68:71]\n" \
    "s_waitcnt lgkmcnt(5)\n" \
    "v_mfma_f32_16x16x32_bf16 v[48:51], v[32:35], v[128:131], v[48:51]\n" \
    "ds_read_b128 v[16:19], v1 offset:40960\n" \
    "s_waitcnt lgkmcnt(5)\n" \
    "v_mfma_f32_16x16x32_bf16 v[52:55], v[36:39], v[128:131], v[52:55]\n" \
    "ds_read_b128 v[20:23], v1 offset:40960\n" \
    "v_mfma_f32_16x16x32_bf16 v[56:59], v[32:35], v[112:115], v[56:59]\n" \
    "ds_read_b128 v[24:27], v1 offset:40960\n" \
    "v_mfma_f32_16x16x32_bf16 v[60:63], v[36:39], v[112:115], v[60:63]\n" \
    "ds_read_b128 v[28:31], v1 offset:40960\n" \
    "v_mfma_f32_16x16x32_bf16 v[64:67], v[32:35], v[96:99], v[64:67]\n" \
    "v_mfma_f32_16x16x32_bf16 v[68:71], v[36:39], v[96:99], v[68:71]\n" \
    "s_waitcnt lgkmcnt(7)\n" \
    "v_mfma_f32_16x16x32_bf16 v[48:51], v[40:43], v[132:135], v[48:51]\n" \
    "s_waitcnt lgkmcnt(6)\n" \
    "v_mfma_f32_16x16x32_bf16 v[52:55], v[44:47], v[132:135], v[52:55]\n" \
    "v_mfma_f32_16x16x32_bf16 v[56:59], v[40:43], v[116:119], v[56:59]\n" \
    "v_mfma_f32_16x16x32_bf16 v[60:63], v[44:47], v[116:119], v[60:63]\n" \
    "v_mfma_f32_16x16x32_bf16 v[64:67], v[40:43], v[100:103], v[64:67]\n" \
    "v_mfma_f32_16x16x32_bf16 v[68:71], v[44:47], v[100:103], v[68:71]\n" \
    "v_mov_b32 v8, v8\n" \
    "v_mov_b32 v8, v8\n" \
    "v_cvt_pk_bf16_f32 v84, v48, v49\n" \
    "v_cvt_pk_bf16_f32 v85, v50, v51\n" \
    "v_cvt_pk_bf16_f32 v86, v52, v53\n" \
    "v_cvt_pk_bf16_f32 v87, v54, v55\n" \
    "v_cvt_pk_bf16_f32 v88, v56, v57\n" \
    "v_cvt_pk_bf16_f32 v89, v58, v59\n" \
    "v_cvt_pk_bf16_f32 v90, v60, v61\n" \
    "v_cvt_pk_bf16_f32 v91, v62, v63\n" \
    "v_cvt_pk_bf16_f32 v92, v64, v65\n" \
    "v_cvt_pk_bf16_f32 v93, v66, v67\n" \
    "v_cvt_pk_bf16_f32 v94, v68, v69\n" \
    "v_cvt_pk_bf16_f32 v95, v70, v71\n" \
    "ds_read_b128 v[48:51], v2\n" \
    "ds_read_b128 v[52:55], v2 offset:1024\n" \
    "ds_read_b128 v[56:59], v2 offset:2048\n" \
    "ds_read_b128 v[60:63], v2 offset:3072\n" \
    "ds_read_b128 v[64:67], v2 offset:16384\n" \
    "ds_read_b128 v[68:71], v2 offset:17408\n" \
    "s_add_u32 s42, s28, 3\n" \
    "s_min_u32 s42, s42, 63\n" \
    "s_cmp_gt_u32 s38, s42\n" \
    "s_cbranch_scc1 KVOKl_3_%=\n" \
    "KVPl_3_%=:\n" \
    "buffer_load_dword v8, off, s[8:11], s24 sc1\n" \
    "s_waitcnt vmcnt(0)\n" \
    "v_readfirstlane_b32 s38, v8\n" \
    "s_nop 0\n" \
    "s_cmp_gt_u32 s38, s42\n" \
    "s_cbranch_scc1 KVOKl_3_%=\n" \
    "s_add_u32 s39, s39, 1\n" \
    "s_cmp_lt_u32 s39, 1048576\n" \
    "s_sleep 1\n" \
    "s_cbranch_scc1 KVPl_3_%=\n" \
    "s_branch FAIL_3_%=\n" \
    "KVOKl_3_%=:\n" \
    "s_add_u32 s29, s29, 262144\n" \
    "s_add_u32 s34, s34, 131072\n" \
    "s_bfe_u32 s40, s26, 0x80018\n" \
    "s_cbranch_scc0 DL1_3_%=X\n" \
    ".p2align 5\n" \
    "DL1_3_%=:\n" \
    "s_nop 0\n" \
    "s_nop 0\n" \
    "s_sub_u32 s40, s40, 1\n" \
    "s_nop 0\n" \
    "s_cmp_gt_u32 s40, 0\n" \
    "s_nop 0\n" \
    "s_cbranch_scc1 DL1_3_%=\n" \
    "DL1_3_%=X:\n" \
    "buffer_load_dwordx4 v[32:35], v10, s[8:11], s31 offen sc1\n" \
    "buffer_load_dwordx4 v[36:39], v10, s[8:11], s31 offen offset:1024 sc1\n" \
    "buffer_load_dwordx4 v[40:43], v10, s[8:11], s31 offen offset:2048 sc1\n" \
    "s_branch GATC_3_%=\n" \
    "GATR_3_%=:\n" \
    "s_add_u32 s39, s39, 1\n" \
    "s_cmp_lt_u32 s39, 1048576\n" \
    "s_cbranch_scc0 FAIL_3_%=\n" \
    "buffer_load_dwordx4 v[32:35], v10, s[8:11], s31 offen sc1\n" \
    "buffer_load_dwordx4 v[36:39], v10, s[8:11], s31 offen offset:1024 sc1\n" \
    "buffer_load_dwordx4 v[40:43], v10, s[8:11], s31 offen offset:2048 sc1\n" \
    "GATC_3_%=:\n" \
    "s_waitcnt vmcnt(2)\n" \
    "ds_write_b128 v11, v[32:35]\n" \
    "v_max3_u32 v44, v32, v33, v34\n" \
    "v_max_u32 v44, v44, v35\n" \
    "s_waitcnt vmcnt(1)\n" \
    "ds_write_b128 v11, v[36:39] offset:1024\n" \
    "v_max3_u32 v44, v44, v36, v37\n" \
    "v_max3_u32 v44, v44, v38, v39\n" \
    "s_waitcnt vmcnt(0)\n" \
    "ds_write_b128 v11, v[40:43] offset:2048\n" \
    "v_max3_u32 v44, v44, v40, v41\n" \
    "v_max3_u32 v44, v44, v42, v43\n" \
    "v_cmp_eq_u32 vcc, -1, v44\n" \
    "s_cbranch_vccnz GATR_3_%=\n" \
    "s_add_u32 s28, s28, 1\n" \
    "s_add_u32 s31, s31, 32768\n" \
    "s_xor_b32 s35, s35, 1\n" \
    "s_not_b32 s36, s36\n" \
    "s_branch LOOP_3_%=\n" \
    "FAIL_3_%=:\n" \
    "v_mov_b32 v72, 1\n" \
    "s_mov_b32 s40, 2560\n" \
    "buffer_store_dword v72, off, s[8:11], s40 sc1\n" \
    "s_branch END_3_%=\n" \
    "LAST_3_%=:\n" \
    "v_mov_b32 v8, v8\n" \
    "v_mov_b32 v8, v8\n" \
    "v_mov_b32 v8, v8\n" \
    "v_mov_b32 v8, v8\n" \
    "v_mov_b32 v8, v8\n" \
    "v_mov_b32 v8, v8\n" \
    "v_mov_b32 v8, v8\n" \
    "v_mov_b32 v8, v8\n" \
    "s_waitcnt lgkmcnt(0)\n" \
    "s_bitcmp1_b32 s26, 0\n" \
    "s_cbranch_scc0 END_3_%=\n" \
    "ds_read_b128 v[72:75], v13\n" \
    "s_waitcnt lgkmcnt(0)\n" \
    "buffer_store_dwordx4 v[72:75], v14, s[8:11], s34 offen sc1\n" \
    "s_waitcnt vmcnt(0)\n" \
    "v_mov_b32 v8, v8\n" \
    "v_mov_b32 v72, 64\n" \
    "buffer_store_dword v72, off, s[8:11], s25 sc1\n" \
    "END_3_%=:\n" \
    "s_waitcnt vmcnt(0) lgkmcnt(0)\n" \
    "s_endpgm\n" \
    ""
#define CHAIN_ASM_2 \
    "s_load_dwordx16 s[68:83], s[8:9], 0x900\n" \
    "global_load_dwordx4 v[72:75], v91, s[20:21]\n" \
    "global_load_dwordx4 v[76:79], v91, s[20:21] offset:128\n" \
    "s_add_u32 s40, s14, 0\n" \
    "s_addc_u32 s41, s15, 0\n" \
    "s_add_u32 m0, s22, 65536\n" \
    "s_nop 0\n" \
    "global_load_lds_dwordx4 v84, s[40:41]\n" \
    "s_add_u32 m0, s22, 66560\n" \
    "s_nop 0\n" \
    "global_load_lds_dwordx4 v85, s[40:41]\n" \
    "s_add_u32 m0, s22, 67584\n" \
    "s_nop 0\n" \
    "global_load_lds_dwordx4 v86, s[40:41]\n" \
    "s_add_u32 m0, s22, 68608\n" \
    "s_nop 0\n" \
    "global_load_lds_dwordx4 v87, s[40:41]\n" \
    "s_add_u32 s40, s16, 0\n" \
    "s_addc_u32 s41, s17, 0\n" \
    "s_add_u32 m0, s22, 81920\n" \
    "s_nop 0\n" \
    "global_load_lds_dwordx4 v84, s[40:41]\n" \
    "s_add_u32 m0, s22, 82944\n" \
    "s_nop 0\n" \
    "global_load_lds_dwordx4 v85, s[40:41]\n" \
    "s_add_u32 m0, s22, 83968\n" \
    "s_nop 0\n" \
    "global_load_lds_dwordx4 v86, s[40:41]\n" \
    "s_add_u32 m0, s22, 84992\n" \
    "s_nop 0\n" \
    "global_load_lds_dwordx4 v87, s[40:41]\n" \
    "s_add_u32 s40, s14, 524288\n" \
    "s_addc_u32 s41, s15, 0\n" \
    "s_add_u32 m0, s22, 98304\n" \
    "s_nop 0\n" \
    "global_load_lds_dwordx4 v84, s[40:41]\n" \
    "s_add_u32 m0, s22, 99328\n" \
    "s_nop 0\n" \
    "global_load_lds_dwordx4 v85, s[40:41]\n" \
    "s_add_u32 m0, s22, 100352\n" \
    "s_nop 0\n" \
    "global_load_lds_dwordx4 v86, s[40:41]\n" \
    "s_add_u32 m0, s22, 101376\n" \
    "s_nop 0\n" \
    "global_load_lds_dwordx4 v87, s[40:41]\n" \
    "s_add_u32 s40, s16, 524288\n" \
    "s_addc_u32 s41, s17, 0\n" \
    "s_add_u32 m0, s22, 114688\n" \
    "s_nop 0\n" \
    "global_load_lds_dwordx4 v84, s[40:41]\n" \
    "s_add_u32 m0, s22, 115712\n" \
    "s_nop 0\n" \
    "global_load_lds_dwordx4 v85, s[40:41]\n" \
    "s_add_u32 m0, s22, 116736\n" \
    "s_nop 0\n" \
    "global_load_lds_dwordx4 v86, s[40:41]\n" \
    "s_add_u32 m0, s22, 117760\n" \
    "s_nop 0\n" \
    "global_load_lds_dwordx4 v87, s[40:41]\n" \
    "s_add_u32 s40, s16, 1048576\n" \
    "s_addc_u32 s41, s17, 0\n" \
    "s_add_u32 m0, s22, 0\n" \
    "s_nop 0\n" \
    "global_load_lds_dwordx4 v84, s[40:41]\n" \
    "s_add_u32 m0, s22, 1024\n" \
    "s_nop 0\n" \
    "global_load_lds_dwordx4 v85, s[40:41]\n" \
    "s_add_u32 m0, s22, 2048\n" \
    "s_nop 0\n" \
    "global_load_lds_dwordx4 v86, s[40:41]\n" \
    "s_add_u32 m0, s22, 3072\n" \
    "s_nop 0\n" \
    "global_load_lds_dwordx4 v87, s[40:41]\n" \
    "s_add_u32 s40, s14, 1572864\n" \
    "s_addc_u32 s41, s15, 0\n" \
    "s_add_u32 m0, s22, 16384\n" \
    "s_nop 0\n" \
    "global_load_lds_dwordx4 v84, s[40:41]\n" \
    "s_add_u32 m0, s22, 17408\n" \
    "s_nop 0\n" \
    "global_load_lds_dwordx4 v85, s[40:41]\n" \
    "s_add_u32 m0, s22, 18432\n" \
    "s_nop 0\n" \
    "global_load_lds_dwordx4 v86, s[40:41]\n" \
    "s_add_u32 m0, s22, 19456\n" \
    "s_nop 0\n" \
    "global_load_lds_dwordx4 v87, s[40:41]\n" \
    "s_add_u32 s40, s16, 1572864\n" \
    "s_addc_u32 s41, s17, 0\n" \
    "s_add_u32 m0, s22, 32768\n" \
    "s_nop 0\n" \
    "global_load_lds_dwordx4 v84, s[40:41]\n" \
    "s_add_u32 m0, s22, 33792\n" \
    "s_nop 0\n" \
    "global_load_lds_dwordx4 v85, s[40:41]\n" \
    "s_add_u32 m0, s22, 34816\n" \
    "s_nop 0\n" \
    "global_load_lds_dwordx4 v86, s[40:41]\n" \
    "s_add_u32 m0, s22, 35840\n" \
    "s_nop 0\n" \
    "global_load_lds_dwordx4 v87, s[40:41]\n" \
    "s_add_u32 s40, s16, 2097152\n" \
    "s_addc_u32 s41, s17, 0\n" \
    "s_add_u32 m0, s22, 49152\n" \
    "s_nop 0\n" \
    "global_load_lds_dwordx4 v84, s[40:41]\n" \
    "s_add_u32 m0, s22, 50176\n" \
    "s_nop 0\n" \
    "global_load_lds_dwordx4 v85, s[40:41]\n" \
    "s_add_u32 m0, s22, 51200\n" \
    "s_nop 0\n" \
    "global_load_lds_dwordx4 v86, s[40:41]\n" \
    "s_add_u32 m0, s22, 52224\n" \
    "s_nop 0\n" \
    "global_load_lds_dwordx4 v87, s[40:41]\n" \
    "s_waitcnt lgkmcnt(0)\n" \
    "s_and_b32 s40, s68, 0xffff\n" \
    "s_lshr_b32 s41, s68, 16\n" \
    "s_and_b32 s46, s69, 0xffff\n" \
    "s_min_u32 s40, s40, s46\n" \
    "s_lshr_b32 s46, s69, 16\n" \
    "s_min_u32 s41, s41, s46\n" \
    "s_and_b32 s46, s70, 0xffff\n" \
    "s_min_u32 s40, s40, s46\n" \
    "s_lshr_b32 s46, s70, 16\n" \
    "s_min_u32 s41, s41, s46\n" \
    "s_and_b32 s46, s71, 0xffff\n" \
    "s_min_u32 s40, s40, s46\n" \
    "s_lshr_b32 s46, s71, 16\n" \
    "s_min_u32 s41, s41, s46\n" \
    "s_and_b32 s46, s72, 0xffff\n" \
    "s_min_u32 s40, s40, s46\n" \
    "s_lshr_b32 s46, s72, 16\n" \
    "s_min_u32 s41, s41, s46\n" \
    "s_and_b32 s46, s73, 0xffff\n" \
    "s_min_u32 s40, s40, s46\n" \
    "s_lshr_b32 s46, s73, 16\n" \
    "s_min_u32 s41, s41, s46\n" \
    "s_and_b32 s46, s74, 0xffff\n" \
    "s_min_u32 s40, s40, s46\n" \
    "s_lshr_b32 s46, s74, 16\n" \
    "s_min_u32 s41, s41, s46\n" \
    "s_and_b32 s46, s75, 0xffff\n" \
    "s_min_u32 s40, s40, s46\n" \
    "s_lshr_b32 s46, s75, 16\n" \
    "s_min_u32 s41, s41, s46\n" \
    "s_cmp_lt_u32 s41, s40\n" \
    "s_cselect_b32 s40, 0x2000, 0\n" \
    "v_or_b32 v9, s40, v9\n" \
    "v_or_b32 v13, s40, v13\n" \
    "s_or_b32 s24, s24, s40\n" \
    "s_or_b32 s25, s25, s40\n" \
    "s_and_b32 s40, s76, 0xffff\n" \
    "s_lshr_b32 s41, s76, 16\n" \
    "s_and_b32 s46, s77, 0xffff\n" \
    "s_min_u32 s40, s40, s46\n" \
    "s_lshr_b32 s46, s77, 16\n" \
    "s_min_u32 s41, s41, s46\n" \
    "s_and_b32 s46, s78, 0xffff\n" \
    "s_min_u32 s40, s40, s46\n" \
    "s_lshr_b32 s46, s78, 16\n" \
    "s_min_u32 s41, s41, s46\n" \
    "s_and_b32 s46, s79, 0xffff\n" \
    "s_min_u32 s40, s40, s46\n" \
    "s_lshr_b32 s46, s79, 16\n" \
    "s_min_u32 s41, s41, s46\n" \
    "s_and_b32 s46, s80, 0xffff\n" \
    "s_min_u32 s40, s40, s46\n" \
    "s_lshr_b32 s46, s80, 16\n" \
    "s_min_u32 s41, s41, s46\n" \
    "s_and_b32 s46, s81, 0xffff\n" \
    "s_min_u32 s40, s40, s46\n" \
    "s_lshr_b32 s46, s81, 16\n" \
    "s_min_u32 s41, s41, s46\n" \
    "s_and_b32 s46, s82, 0xffff\n" \
    "s_min_u32 s40, s40, s46\n" \
    "s_lshr_b32 s46, s82, 16\n" \
    "s_min_u32 s41, s41, s46\n" \
    "s_and_b32 s46, s83, 0xffff\n" \
    "s_min_u32 s40, s40, s46\n" \
    "s_lshr_b32 s46, s83, 16\n" \
    "s_min_u32 s41, s41, s46\n" \
    "s_cmp_lt_u32 s41, s40\n" \
    "s_cselect_b32 s40, 0x2000, 0\n" \
    "v_or_b32 v10, s40, v10\n" \
    "v_or_b32 v14, s40, v14\n" \
    "v_add_u32 v92, 0x10000, v88\n" \
    "v_add_u32 v93, 0x10000, v89\n" \
    "v_mov_b32 v32, 0\n" \
    "v_mov_b32 v33, 0\n" \
    "v_mov_b32 v34, 0\n" \
    "v_mov_b32 v35, 0\n" \
    "ds_write_b128 v1, v[32:35] offset:40960\n" \
    "v_mov_b32 v36, 0x3f803f80\n" \
    "v_mov_b32 v37, 0x3f803f80\n" \
    "v_mov_b32 v38, 0x3f803f80\n" \
    "v_mov_b32 v39, 0x3f803f80\n" \
    "ds_write_b128 v1, v[36:39] offset:41984\n" \
    "v_mov_b32 v40, 0x3f003f00\n" \
    "v_mov_b32 v41, 0x3f003f00\n" \
    "v_mov_b32 v42, 0x3f003f00\n" \
    "v_mov_b32 v43, 0x3f003f00\n" \
    "ds_write_b128 v1, v[40:43] offset:43008\n" \
    "v_lshrrev_b32 v48, 4, v1\n" \
    "v_and_b32 v49, 1, v48\n" \
    "v_lshlrev_b32 v49, 4, v49\n" \
    "v_mov_b32 v50, 0x3f80\n" \
    "v_lshlrev_b32 v50, v49, v50\n" \
    "v_bfe_u32 v48, v48, 1, 2\n" \
    "v_cmp_eq_u32 vcc, 0, v48\n" \
    "s_nop 1\n" \
    "v_cndmask_b32 v44, 0, v50, vcc\n" \
    "v_cmp_eq_u32 vcc, 1, v48\n" \
    "s_nop 1\n" \
    "v_cndmask_b32 v45, 0, v50, vcc\n" \
    "v_cmp_eq_u32 vcc, 2, v48\n" \
    "s_nop 1\n" \
    "v_cndmask_b32 v46, 0, v50, vcc\n" \
    "v_cmp_eq_u32 vcc, 3, v48\n" \
    "s_nop 1\n" \
    "v_cndmask_b32 v47, 0, v50, vcc\n" \
    "ds_write_b128 v1, v[44:47] offset:44032\n" \
    "s_mov_b32 s27, 0x3e0293ee\n" \
    "s_mov_b32 s28, 0\n" \
    "s_mov_b32 s29, 0xa40000\n" \
    "s_mov_b32 s30, 0x1b80000\n" \
    "s_mov_b32 s31, 0x408000\n" \
    "s_mov_b32 s34, 0x2c00000\n" \
    "s_mov_b32 s35, 0\n" \
    "s_mov_b32 s36, 0\n" \
    "s_add_u32 s37, s22, 133120\n" \
    "s_add_u32 s43, s22, 124928\n" \
    "s_mov_b32 s38, 0\n" \
    "s_mov_b32 s39, 0\n" \
    "s_mov_b32 s44, 0x00ff00ff\n" \
    "s_mov_b32 s45, 0x00ff00ff\n" \
    "s_mov_b32 s42, 2\n" \
    "s_waitcnt vmcnt(16)\n" \
    "s_waitcnt lgkmcnt(0)\n" \
    "s_barrier\n" \
    "ds_read_b64_tr_b16 a[0:1], v92\n" \
    "ds_read_b64_tr_b16 a[2:3], v92 offset:2048\n" \
    "ds_read_b64_tr_b16 a[4:5], v92 offset:8192\n" \
    "ds_read_b64_tr_b16 a[6:7], v92 offset:10240\n" \
    "ds_read_b64_tr_b16 a[8:9], v93\n" \
    "ds_read_b64_tr_b16 a[10:11], v93 offset:2048\n" \
    "ds_read_b64_tr_b16 a[12:13], v93 offset:8192\n" \
    "ds_read_b64_tr_b16 a[14:15], v93 offset:10240\n" \
    "ds_read_b64_tr_b16 a[16:17], v92 offset:16384\n" \
    "ds_read_b64_tr_b16 a[18:19], v92 offset:18432\n" \
    "ds_read_b64_tr_b16 a[20:21], v92 offset:24576\n" \
    "ds_read_b64_tr_b16 a[22:23], v92 offset:26624\n" \
    "ds_read_b64_tr_b16 a[24:25], v93 offset:16384\n" \
    "ds_read_b64_tr_b16 a[26:27], v93 offset:18432\n" \
    "ds_read_b64_tr_b16 a[28:29], v93 offset:24576\n" \
    "ds_read_b64_tr_b16 a[30:31], v93 offset:26624\n" \
    "ds_read_b64_tr_b16 a[48:49], v92 offset:32768\n" \
    "ds_read_b64_tr_b16 a[50:51], v92 offset:34816\n" \
    "ds_read_b64_tr_b16 a[52:53], v92 offset:40960\n" \
    "ds_read_b64_tr_b16 a[54:55], v92 offset:43008\n" \
    "ds_read_b64_tr_b16 a[56:57], v93 offset:32768\n" \
    "ds_read_b64_tr_b16 a[58:59], v93 offset:34816\n" \
    "ds_read_b64_tr_b16 a[60:61], v93 offset:40960\n" \
    "ds_read_b64_tr_b16 a[62:63], v93 offset:43008\n" \
    "ds_read_b64_tr_b16 a[64:65], v92 offset:49152\n" \
    "ds_read_b64_tr_b16 a[66:67], v92 offset:51200\n" \
    "ds_read_b64_tr_b16 a[68:69], v92 offset:57344\n" \
    "ds_read_b64_tr_b16 a[70:71], v92 offset:59392\n" \
    "ds_read_b64_tr_b16 a[72:73], v93 offset:49152\n" \
    "ds_read_b64_tr_b16 a[74:75], v93 offset:51200\n" \
    "ds_read_b64_tr_b16 a[76:77], v93 offset:57344\n" \
    "ds_read_b64_tr_b16 a[78:79], v93 offset:59392\n" \
    "s_waitcnt lgkmcnt(0)\n" \
    "s_barrier\n" \
    "s_add_u32 s40, s14, 2621440\n" \
    "s_addc_u32 s41, s15, 0\n" \
    "s_add_u32 m0, s22, 65536\n" \
    "s_nop 0\n" \
    "global_load_lds_dwordx4 v84, s[40:41]\n" \
    "s_add_u32 m0, s22, 66560\n" \
    "s_nop 0\n" \
    "global_load_lds_dwordx4 v85, s[40:41]\n" \
    "s_add_u32 m0, s22, 67584\n" \
    "s_nop 0\n" \
    "global_load_lds_dwordx4 v86, s[40:41]\n" \
    "s_add_u32 m0, s22, 68608\n" \
    "s_nop 0\n" \
    "global_load_lds_dwordx4 v87, s[40:41]\n" \
    "s_add_u32 s40, s16, 2621440\n" \
    "s_addc_u32 s41, s17, 0\n" \
    "s_add_u32 m0, s22, 81920\n" \
    "s_nop 0\n" \
    "global_load_lds_dwordx4 v84, s[40:41]\n" \
    "s_add_u32 m0, s22, 82944\n" \
    "s_nop 0\n" \
    "global_load_lds_dwordx4 v85, s[40:41]\n" \
    "s_add_u32 m0, s22, 83968\n" \
    "s_nop 0\n" \
    "global_load_lds_dwordx4 v86, s[40:41]\n" \
    "s_add_u32 m0, s22, 84992\n" \
    "s_nop 0\n" \
    "global_load_lds_dwordx4 v87, s[40:41]\n" \
    "s_add_u32 s40, s14, 3145728\n" \
    "s_addc_u32 s41, s15, 0\n" \
    "s_add_u32 m0, s22, 98304\n" \
    "s_nop 0\n" \
    "global_load_lds_dwordx4 v84, s[40:41]\n" \
    "s_add_u32 m0, s22, 99328\n" \
    "s_nop 0\n" \
    "global_load_lds_dwordx4 v85, s[40:41]\n" \
    "s_add_u32 m0, s22, 100352\n" \
    "s_nop 0\n" \
    "global_load_lds_dwordx4 v86, s[40:41]\n" \
    "s_add_u32 m0, s22, 101376\n" \
    "s_nop 0\n" \
    "global_load_lds_dwordx4 v87, s[40:41]\n" \
    "s_add_u32 s40, s16, 3145728\n" \
    "s_addc_u32 s41, s17, 0\n" \
    "s_add_u32 m0, s22, 114688\n" \
    "s_nop 0\n" \
    "global_load_lds_dwordx4 v84, s[40:41]\n" \
    "s_add_u32 m0, s22, 115712\n" \
    "s_nop 0\n" \
    "global_load_lds_dwordx4 v85, s[40:41]\n" \
    "s_add_u32 m0, s22, 116736\n" \
    "s_nop 0\n" \
    "global_load_lds_dwordx4 v86, s[40:41]\n" \
    "s_add_u32 m0, s22, 117760\n" \
    "s_nop 0\n" \
    "global_load_lds_dwordx4 v87, s[40:41]\n" \
    "s_waitcnt vmcnt(16)\n" \
    "s_waitcnt lgkmcnt(0)\n" \
    "s_barrier\n" \
    "ds_read_b64_tr_b16 a[96:97], v88\n" \
    "ds_read_b64_tr_b16 a[98:99], v88 offset:2048\n" \
    "ds_read_b64_tr_b16 a[100:101], v88 offset:8192\n" \
    "ds_read_b64_tr_b16 a[102:103], v88 offset:10240\n" \
    "ds_read_b64_tr_b16 a[104:105], v89\n" \
    "ds_read_b64_tr_b16 a[106:107], v89 offset:2048\n" \
    "ds_read_b64_tr_b16 a[108:109], v89 offset:8192\n" \
    "ds_read_b64_tr_b16 a[110:111], v89 offset:10240\n" \
    "ds_read_b64_tr_b16 a[112:113], v88 offset:16384\n" \
    "ds_read_b64_tr_b16 a[114:115], v88 offset:18432\n" \
    "ds_read_b64_tr_b16 a[116:117], v88 offset:24576\n" \
    "ds_read_b64_tr_b16 a[118:119], v88 offset:26624\n" \
    "ds_read_b64_tr_b16 a[120:121], v89 offset:16384\n" \
    "ds_read_b64_tr_b16 a[122:123], v89 offset:18432\n" \
    "ds_read_b64_tr_b16 a[124:125], v89 offset:24576\n" \
    "ds_read_b64_tr_b16 a[126:127], v89 offset:26624\n" \
    "ds_read_b64_tr_b16 a[128:129], v88 offset:32768\n" \
    "ds_read_b64_tr_b16 a[130:131], v88 offset:34816\n" \
    "ds_read_b64_tr_b16 a[132:133], v88 offset:40960\n" \
    "ds_read_b64_tr_b16 a[134:135], v88 offset:43008\n" \
    "ds_read_b64_tr_b16 a[136:137], v89 offset:32768\n" \
    "ds_read_b64_tr_b16 a[138:139], v89 offset:34816\n" \
    "ds_read_b64_tr_b16 a[140:141], v89 offset:40960\n" \
    "ds_read_b64_tr_b16 a[142:143], v89 offset:43008\n" \
    "ds_read_b64_tr_b16 a[160:161], v88 offset:49152\n" \
    "ds_read_b64_tr_b16 a[162:163], v88 offset:51200\n" \
    "ds_read_b64_tr_b16 a[164:165], v88 offset:57344\n" \
    "ds_read_b64_tr_b16 a[166:167], v88 offset:59392\n" \
    "ds_read_b64_tr_b16 a[168:169], v89 offset:49152\n" \
    "ds_read_b64_tr_b16 a[170:171], v89 offset:51200\n" \
    "ds_read_b64_tr_b16 a[172:173], v89 offset:57344\n" \
    "ds_read_b64_tr_b16 a[174:175], v89 offset:59392\n" \
    "s_waitcnt lgkmcnt(0)\n" \
    "s_barrier\n" \
    "s_add_u32 s40, s16, 3670016\n" \
    "s_addc_u32 s41, s17, 0\n" \
    "s_add_u32 m0, s22, 0\n" \
    "s_nop 0\n" \
    "global_load_lds_dwordx4 v84, s[40:41]\n" \
    "s_add_u32 m0, s22, 1024\n" \
    "s_nop 0\n" \
    "global_load_lds_dwordx4 v85, s[40:41]\n" \
    "s_add_u32 m0, s22, 2048\n" \
    "s_nop 0\n" \
    "global_load_lds_dwordx4 v86, s[40:41]\n" \
    "s_add_u32 m0, s22, 3072\n" \
    "s_nop 0\n" \
    "global_load_lds_dwordx4 v87, s[40:41]\n" \
    "s_add_u32 s40, s16, 4194304\n" \
    "s_addc_u32 s41, s17, 0\n" \
    "s_add_u32 m0, s22, 16384\n" \
    "s_nop 0\n" \
    "global_load_lds_dwordx4 v84, s[40:41]\n" \
    "s_add_u32 m0, s22, 17408\n" \
    "s_nop 0\n" \
    "global_load_lds_dwordx4 v85, s[40:41]\n" \
    "s_add_u32 m0, s22, 18432\n" \
    "s_nop 0\n" \
    "global_load_lds_dwordx4 v86, s[40:41]\n" \
    "s_add_u32 m0, s22, 19456\n" \
    "s_nop 0\n" \
    "global_load_lds_dwordx4 v87, s[40:41]\n" \
    "s_add_u32 s40, s14, 4718592\n" \
    "s_addc_u32 s41, s15, 0\n" \
    "s_add_u32 m0, s22, 32768\n" \
    "s_nop 0\n" \
    "global_load_lds_dwordx4 v84, s[40:41]\n" \
    "s_add_u32 m0, s22, 33792\n" \
    "s_nop 0\n" \
    "global_load_lds_dwordx4 v85, s[40:41]\n" \
    "s_add_u32 m0, s22, 34816\n" \
    "s_nop 0\n" \
    "global_load_lds_dwordx4 v86, s[40:41]\n" \
    "s_add_u32 m0, s22, 35840\n" \
    "s_nop 0\n" \
    "global_load_lds_dwordx4 v87, s[40:41]\n" \
    "s_add_u32 s40, s16, 4718592\n" \
    "s_addc_u32 s41, s17, 0\n" \
    "s_add_u32 m0, s22, 49152\n" \
    "s_nop 0\n" \
    "global_load_lds_dwordx4 v84, s[40:41]\n" \
    "s_add_u32 m0, s22, 50176\n" \
    "s_nop 0\n" \
    "global_load_lds_dwordx4 v85, s[40:41]\n" \
    "s_add_u32 m0, s22, 51200\n" \
    "s_nop 0\n" \
    "global_load_lds_dwordx4 v86, s[40:41]\n" \
    "s_add_u32 m0, s22, 52224\n" \
    "s_nop 0\n" \
    "global_load_lds_dwordx4 v87, s[40:41]\n" \
    "s_waitcnt vmcnt(16)\n" \
    "s_waitcnt lgkmcnt(0)\n" \
    "s_barrier\n" \
    "ds_read_b64_tr_b16 a[192:193], v92\n" \
    "ds_read_b64_tr_b16 a[194:195], v92 offset:2048\n" \
    "ds_read_b64_tr_b16 a[196:197], v92 offset:8192\n" \
    "ds_read_b64_tr_b16 a[198:199], v92 offset:10240\n" \
    "ds_read_b64_tr_b16 a[200:201], v93\n" \
    "ds_read_b64_tr_b16 a[202:203], v93 offset:2048\n" \
    "ds_read_b64_tr_b16 a[204:205], v93 offset:8192\n" \
    "ds_read_b64_tr_b16 a[206:207], v93 offset:10240\n" \
    "ds_read_b64_tr_b16 a[208:209], v92 offset:16384\n" \
    "ds_read_b64_tr_b16 a[210:211], v92 offset:18432\n" \
    "ds_read_b64_tr_b16 a[212:213], v92 offset:24576\n" \
    "ds_read_b64_tr_b16 a[214:215], v92 offset:26624\n" \
    "ds_read_b64_tr_b16 a[216:217], v93 offset:16384\n" \
    "ds_read_b64_tr_b16 a[218:219], v93 offset:18432\n" \
    "ds_read_b64_tr_b16 a[220:221], v93 offset:24576\n" \
    "ds_read_b64_tr_b16 a[222:223], v93 offset:26624\n" \
    "ds_read_b64_tr_b16 a[224:225], v92 offset:32768\n" \
    "ds_read_b64_tr_b16 a[226:227], v92 offset:34816\n" \
    "ds_read_b64_tr_b16 a[228:229], v92 offset:40960\n" \
    "ds_read_b64_tr_b16 a[230:231], v92 offset:43008\n" \
    "ds_read_b64_tr_b16 a[232:233], v93 offset:32768\n" \
    "ds_read_b64_tr_b16 a[234:235], v93 offset:34816\n" \
    "ds_read_b64_tr_b16 a[236:237], v93 offset:40960\n" \
    "ds_read_b64_tr_b16 a[238:239], v93 offset:43008\n" \
    "ds_read_b64_tr_b16 a[240:241], v92 offset:49152\n" \
    "ds_read_b64_tr_b16 a[242:243], v92 offset:51200\n" \
    "ds_read_b64_tr_b16 a[244:245], v92 offset:57344\n" \
    "ds_read_b64_tr_b16 a[246:247], v92 offset:59392\n" \
    "ds_read_b64_tr_b16 a[248:249], v93 offset:49152\n" \
    "ds_read_b64_tr_b16 a[250:251], v93 offset:51200\n" \
    "ds_read_b64_tr_b16 a[252:253], v93 offset:57344\n" \
    "ds_read_b64_tr_b16 a[254:255], v93 offset:59392\n" \
    "s_waitcnt lgkmcnt(0)\n" \
    "s_barrier\n" \
    "s_add_u32 s40, s12, 524288\n" \
    "s_addc_u32 s41, s13, 0\n" \
    "s_add_u32 m0, s22, 65536\n" \
    "s_nop 0\n" \
    "global_load_lds_dwordx4 v84, s[40:41]\n" \
    "s_add_u32 m0, s22, 66560\n" \
    "s_nop 0\n" \
    "global_load_lds_dwordx4 v85, s[40:41]\n" \
    "s_add_u32 m0, s22, 67584\n" \
    "s_nop 0\n" \
    "global_load_lds_dwordx4 v86, s[40:41]\n" \
    "s_add_u32 m0, s22, 68608\n" \
    "s_nop 0\n" \
    "global_load_lds_dwordx4 v87, s[40:41]\n" \
    "s_add_u32 s40, s12, 1048576\n" \
    "s_addc_u32 s41, s13, 0\n" \
    "s_add_u32 m0, s22, 81920\n" \
    "s_nop 0\n" \
    "global_load_lds_dwordx4 v84, s[40:41]\n" \
    "s_add_u32 m0, s22, 82944\n" \
    "s_nop 0\n" \
    "global_load_lds_dwordx4 v85, s[40:41]\n" \
    "s_add_u32 m0, s22, 83968\n" \
    "s_nop 0\n" \
    "global_load_lds_dwordx4 v86, s[40:41]\n" \
    "s_add_u32 m0, s22, 84992\n" \
    "s_nop 0\n" \
    "global_load_lds_dwordx4 v87, s[40:41]\n" \
    "s_add_u32 s40, s12, 2097152\n" \
    "s_addc_u32 s41, s13, 0\n" \
    "s_add_u32 m0, s22, 98304\n" \
    "s_nop 0\n" \
    "global_load_lds_dwordx4 v84, s[40:41]\n" \
    "s_add_u32 m0, s22, 99328\n" \
    "s_nop 0\n" \
    "global_load_lds_dwordx4 v85, s[40:41]\n" \
    "s_add_u32 m0, s22, 100352\n" \
    "s_nop 0\n" \
    "global_load_lds_dwordx4 v86, s[40:41]\n" \
    "s_add_u32 m0, s22, 101376\n" \
    "s_nop 0\n" \
    "global_load_lds_dwordx4 v87, s[40:41]\n" \
    "s_add_u32 s40, s12, 2621440\n" \
    "s_addc_u32 s41, s13, 0\n" \
    "s_add_u32 m0, s22, 114688\n" \
    "s_nop 0\n" \
    "global_load_lds_dwordx4 v84, s[40:41]\n" \
    "s_add_u32 m0, s22, 115712\n" \
    "s_nop 0\n" \
    "global_load_lds_dwordx4 v85, s[40:41]\n" \
    "s_add_u32 m0, s22, 116736\n" \
    "s_nop 0\n" \
    "global_load_lds_dwordx4 v86, s[40:41]\n" \
    "s_add_u32 m0, s22, 117760\n" \
    "s_nop 0\n" \
    "global_load_lds_dwordx4 v87, s[40:41]\n" \
    "s_waitcnt vmcnt(16)\n" \
    "s_waitcnt lgkmcnt(0)\n" \
    "s_barrier\n" \
    "ds_read_b64_tr_b16 v[232:233], v88\n" \
    "ds_read_b64_tr_b16 v[234:235], v88 offset:2048\n" \
    "ds_read_b64_tr_b16 v[236:237], v88 offset:8192\n" \
    "ds_read_b64_tr_b16 v[238:239], v88 offset:10240\n" \
    "ds_read_b64_tr_b16 v[224:225], v89\n" \
    "ds_read_b64_tr_b16 v[226:227], v89 offset:2048\n" \
    "ds_read_b64_tr_b16 v[228:229], v89 offset:8192\n" \
    "ds_read_b64_tr_b16 v[230:231], v89 offset:10240\n" \
    "ds_read_b64_tr_b16 v[200:201], v88 offset:16384\n" \
    "ds_read_b64_tr_b16 v[202:203], v88 offset:18432\n" \
    "ds_read_b64_tr_b16 v[204:205], v88 offset:24576\n" \
    "ds_read_b64_tr_b16 v[206:207], v88 offset:26624\n" \
    "ds_read_b64_tr_b16 v[192:193], v89 offset:16384\n" \
    "ds_read_b64_tr_b16 v[194:195], v89 offset:18432\n" \
    "ds_read_b64_tr_b16 v[196:197], v89 offset:24576\n" \
    "ds_read_b64_tr_b16 v[198:199], v89 offset:26624\n" \
    "ds_read_b64_tr_b16 v[168:169], v88 offset:32768\n" \
    "ds_read_b64_tr_b16 v[170:171], v88 offset:34816\n" \
    "ds_read_b64_tr_b16 v[172:173], v88 offset:40960\n" \
    "ds_read_b64_tr_b16 v[174:175], v88 offset:43008\n" \
    "ds_read_b64_tr_b16 v[160:161], v89 offset:32768\n" \
    "ds_read_b64_tr_b16 v[162:163], v89 offset:34816\n" \
    "ds_read_b64_tr_b16 v[164:165], v89 offset:40960\n" \
    "ds_read_b64_tr_b16 v[166:167], v89 offset:43008\n" \
    "ds_read_b64_tr_b16 v[152:153], v88 offset:49152\n" \
    "ds_read_b64_tr_b16 v[154:155], v88 offset:51200\n" \
    "ds_read_b64_tr_b16 v[156:157], v88 offset:57344\n" \
    "ds_read_b64_tr_b16 v[158:159], v88 offset:59392\n" \
    "ds_read_b64_tr_b16 v[144:145], v89 offset:49152\n" \
    "ds_read_b64_tr_b16 v[146:147], v89 offset:51200\n" \
    "ds_read_b64_tr_b16 v[148:149], v89 offset:57344\n" \
    "ds_read_b64_tr_b16 v[150:151], v89 offset:59392\n" \
    "s_waitcnt lgkmcnt(0)\n" \
    "s_barrier\n" \
    "s_add_u32 s40, s12, 3670016\n" \
    "s_addc_u32 s41, s13, 0\n" \
    "s_add_u32 m0, s22, 0\n" \
    "s_nop 0\n" \
    "global_load_lds_dwordx4 v84, s[40:41]\n" \
    "s_add_u32 m0, s22, 1024\n" \
    "s_nop 0\n" \
    "global_load_lds_dwordx4 v85, s[40:41]\n" \
    "s_add_u32 m0, s22, 2048\n" \
    "s_nop 0\n" \
    "global_load_lds_dwordx4 v86, s[40:41]\n" \
    "s_add_u32 m0, s22, 3072\n" \
    "s_nop 0\n" \
    "global_load_lds_dwordx4 v87, s[40:41]\n" \
    "s_add_u32 s40, s12, 4194304\n" \
    "s_addc_u32 s41, s13, 0\n" \
    "s_add_u32 m0, s22, 16384\n" \
    "s_nop 0\n" \
    "global_load_lds_dwordx4 v84, s[40:41]\n" \
    "s_add_u32 m0, s22, 17408\n" \
    "s_nop 0\n" \
    "global_load_lds_dwordx4 v85, s[40:41]\n" \
    "s_add_u32 m0, s22, 18432\n" \
    "s_nop 0\n" \
    "global_load_lds_dwordx4 v86, s[40:41]\n" \
    "s_add_u32 m0, s22, 19456\n" \
    "s_nop 0\n" \
    "global_load_lds_dwordx4 v87, s[40:41]\n" \
    "s_add_u32 s40, s12, 4718592\n" \
    "s_addc_u32 s41, s13, 0\n" \
    "s_add_u32 m0, s22, 32768\n" \
    "s_nop 0\n" \
    "global_load_lds_dwordx4 v84, s[40:41]\n" \
    "s_add_u32 m0, s22, 33792\n" \
    "s_nop 0\n" \
    "global_load_lds_dwordx4 v85, s[40:41]\n" \
    "s_add_u32 m0, s22, 34816\n" \
    "s_nop 0\n" \
    "global_load_lds_dwordx4 v86, s[40:41]\n" \
    "s_add_u32 m0, s22, 35840\n" \
    "s_nop 0\n" \
    "global_load_lds_dwordx4 v87, s[40:41]\n" \
    "s_waitcnt vmcnt(12)\n" \
    "s_waitcnt lgkmcnt(0)\n" \
    "s_barrier\n" \
    "ds_read_b64_tr_b16 a[32:33], v92\n" \
    "ds_read_b64_tr_b16 a[34:35], v92 offset:2048\n" \
    "ds_read_b64_tr_b16 a[36:37], v92 offset:8192\n" \
    "ds_read_b64_tr_b16 a[38:39], v92 offset:10240\n" \
    "ds_read_b64_tr_b16 a[40:41], v93\n" \
    "ds_read_b64_tr_b16 a[42:43], v93 offset:2048\n" \
    "ds_read_b64_tr_b16 a[44:45], v93 offset:8192\n" \
    "ds_read_b64_tr_b16 a[46:47], v93 offset:10240\n" \
    "ds_read_b64_tr_b16 a[80:81], v92 offset:16384\n" \
    "ds_read_b64_tr_b16 a[82:83], v92 offset:18432\n" \
    "ds_read_b64_tr_b16 a[84:85], v92 offset:24576\n" \
    "ds_read_b64_tr_b16 a[86:87], v92 offset:26624\n" \
    "ds_read_b64_tr_b16 a[88:89], v93 offset:16384\n" \
    "ds_read_b64_tr_b16 a[90:91], v93 offset:18432\n" \
    "ds_read_b64_tr_b16 a[92:93], v93 offset:24576\n" \
    "ds_read_b64_tr_b16 a[94:95], v93 offset:26624\n" \
    "ds_read_b64_tr_b16 a[144:145], v92 offset:32768\n" \
    "ds_read_b64_tr_b16 a[146:147], v92 offset:34816\n" \
    "ds_read_b64_tr_b16 a[148:149], v92 offset:40960\n" \
    "ds_read_b64_tr_b16 a[150:151], v92 offset:43008\n" \
    "ds_read_b64_tr_b16 a[152:153], v93 offset:32768\n" \
    "ds_read_b64_tr_b16 a[154:155], v93 offset:34816\n" \
    "ds_read_b64_tr_b16 a[156:157], v93 offset:40960\n" \
    "ds_read_b64_tr_b16 a[158:159], v93 offset:43008\n" \
    "ds_read_b64_tr_b16 a[176:177], v92 offset:49152\n" \
    "ds_read_b64_tr_b16 a[178:179], v92 offset:51200\n" \
    "ds_read_b64_tr_b16 a[180:181], v92 offset:57344\n" \
    "ds_read_b64_tr_b16 a[182:183], v92 offset:59392\n" \
    "ds_read_b64_tr_b16 a[184:185], v93 offset:49152\n" \
    "ds_read_b64_tr_b16 a[186:187], v93 offset:51200\n" \
    "ds_read_b64_tr_b16 a[188:189], v93 offset:57344\n" \
    "ds_read_b64_tr_b16 a[190:191], v93 offset:59392\n" \
    "s_waitcnt lgkmcnt(0)\n" \
    "s_barrier\n" \
    "ds_write_b128 v11, v[72:75]\n" \
    "ds_write_b128 v11, v[76:79] offset:1024\n" \
    "s_add_u32 m0, s23, 65536\n" \
    "s_nop 0\n" \
    "global_load_lds_dwordx4 v16, s[18:19]\n" \
    "s_add_u32 m0, s23, 66560\n" \
    "s_nop 0\n" \
    "global_load_lds_dwordx4 v17, s[18:19]\n" \
    "s_add_u32 m0, s23, 67584\n" \
    "s_nop 0\n" \
    "global_load_lds_dwordx4 v18, s[18:19]\n" \
    "s_add_u32 m0, s23, 68608\n" \
    "s_nop 0\n" \
    "global_load_lds_dwordx4 v19, s[18:19]\n" \
    "s_add_u32 m0, s23, 69632\n" \
    "s_nop 0\n" \
    "global_load_lds_dwordx4 v20, s[18:19]\n" \
    "s_add_u32 m0, s23, 70656\n" \
    "s_nop 0\n" \
    "global_load_lds_dwordx4 v21, s[18:19]\n" \
    "s_add_u32 m0, s23, 71680\n" \
    "s_nop 0\n" \
    "global_load_lds_dwordx4 v22, s[18:19]\n" \
    "s_add_u32 m0, s23, 72704\n" \
    "s_nop 0\n" \
    "global_load_lds_dwordx4 v23, s[18:19]\n" \
    "s_add_u32 m0, s23, 73728\n" \
    "s_nop 0\n" \
    "global_load_lds_dwordx4 v24, s[18:19]\n" \
    "s_add_u32 m0, s23, 74752\n" \
    "s_nop 0\n" \
    "global_load_lds_dwordx4 v25, s[18:19]\n" \
    "s_waitcnt vmcnt(10)\n" \
    "s_waitcnt lgkmcnt(0)\n" \
    "s_barrier\n" \
    "ds_read_b64_tr_b16 v[248:249], v88\n" \
    "ds_read_b64_tr_b16 v[250:251], v88 offset:2048\n" \
    "ds_read_b64_tr_b16 v[252:253], v88 offset:8192\n" \
    "ds_read_b64_tr_b16 v[254:255], v88 offset:10240\n" \
    "ds_read_b64_tr_b16 v[240:241], v89\n" \
    "ds_read_b64_tr_b16 v[242:243], v89 offset:2048\n" \
    "ds_read_b64_tr_b16 v[244:245], v89 offset:8192\n" \
    "ds_read_b64_tr_b16 v[246:247], v89 offset:10240\n" \
    "ds_read_b64_tr_b16 v[216:217], v88 offset:16384\n" \
    "ds_read_b64_tr_b16 v[218:219], v88 offset:18432\n" \
    "ds_read_b64_tr_b16 v[220:221], v88 offset:24576\n" \
    "ds_read_b64_tr_b16 v[222:223], v88 offset:26624\n" \
    "ds_read_b64_tr_b16 v[208:209], v89 offset:16384\n" \
    "ds_read_b64_tr_b16 v[210:211], v89 offset:18432\n" \
    "ds_read_b64_tr_b16 v[212:213], v89 offset:24576\n" \
    "ds_read_b64_tr_b16 v[214:215], v89 offset:26624\n" \
    "ds_read_b64_tr_b16 v[184:185], v88 offset:32768\n" \
    "ds_read_b64_tr_b16 v[186:187], v88 offset:34816\n" \
    "ds_read_b64_tr_b16 v[188:189], v88 offset:40960\n" \
    "ds_read_b64_tr_b16 v[190:191], v88 offset:43008\n" \
    "ds_read_b64_tr_b16 v[176:177], v89 offset:32768\n" \
    "ds_read_b64_tr_b16 v[178:179], v89 offset:34816\n" \
    "ds_read_b64_tr_b16 v[180:181], v89 offset:40960\n" \
    "ds_read_b64_tr_b16 v[182:183], v89 offset:43008\n" \
    "s_waitcnt lgkmcnt(0)\n" \
    "s_barrier\n" \
    "s_add_u32 s40, s12, 0\n" \
    "s_addc_u32 s41, s13, 0\n" \
    "s_add_u32 m0, s22, 0\n" \
    "s_nop 0\n" \
    "global_load_lds_dwordx4 v84, s[40:41]\n" \
    "s_add_u32 m0, s22, 1024\n" \
    "s_nop 0\n" \
    "global_load_lds_dwordx4 v85, s[40:41]\n" \
    "s_add_u32 m0, s22, 2048\n" \
    "s_nop 0\n" \
    "global_load_lds_dwordx4 v86, s[40:41]\n" \
    "s_add_u32 m0, s22, 3072\n" \
    "s_nop 0\n" \
    "global_load_lds_dwordx4 v87, s[40:41]\n" \
    "s_add_u32 s40, s14, 1048576\n" \
    "s_addc_u32 s41, s15, 0\n" \
    "s_add_u32 m0, s22, 16384\n" \
    "s_nop 0\n" \
    "global_load_lds_dwordx4 v84, s[40:41]\n" \
    "s_add_u32 m0, s22, 17408\n" \
    "s_nop 0\n" \
    "global_load_lds_dwordx4 v85, s[40:41]\n" \
    "s_add_u32 m0, s22, 18432\n" \
    "s_nop 0\n" \
    "global_load_lds_dwordx4 v86, s[40:41]\n" \
    "s_add_u32 m0, s22, 19456\n" \
    "s_nop 0\n" \
    "global_load_lds_dwordx4 v87, s[40:41]\n" \
    "s_add_u32 s40, s12, 1572864\n" \
    "s_addc_u32 s41, s13, 0\n" \
    "s_add_u32 m0, s22, 32768\n" \
    "s_nop 0\n" \
    "global_load_lds_dwordx4 v84, s[40:41]\n" \
    "s_add_u32 m0, s22, 33792\n" \
    "s_nop 0\n" \
    "global_load_lds_dwordx4 v85, s[40:41]\n" \
    "s_add_u32 m0, s22, 34816\n" \
    "s_nop 0\n" \
    "global_load_lds_dwordx4 v86, s[40:41]\n" \
    "s_add_u32 m0, s22, 35840\n" \
    "s_nop 0\n" \
    "global_load_lds_dwordx4 v87, s[40:41]\n" \
    "s_add_u32 s40, s14, 2097152\n" \
    "s_addc_u32 s41, s15, 0\n" \
    "s_add_u32 m0, s22, 49152\n" \
    "s_nop 0\n" \
    "global_load_lds_dwordx4 v84, s[40:41]\n" \
    "s_add_u32 m0, s22, 50176\n" \
    "s_nop 0\n" \
    "global_load_lds_dwordx4 v85, s[40:41]\n" \
    "s_add_u32 m0, s22, 51200\n" \
    "s_nop 0\n" \
    "global_load_lds_dwordx4 v86, s[40:41]\n" \
    "s_add_u32 m0, s22, 52224\n" \
    "s_nop 0\n" \
    "global_load_lds_dwordx4 v87, s[40:41]\n" \
    "s_waitcnt vmcnt(16)\n" \
    "s_waitcnt lgkmcnt(0)\n" \
    "s_barrier\n" \
    "ds_read_b64_tr_b16 v[136:137], v90\n" \
    "ds_read_b64_tr_b16 v[138:139], v90 offset:5120\n" \
    "ds_read_b64_tr_b16 v[140:141], v90 offset:10240\n" \
    "ds_read_b64_tr_b16 v[142:143], v90 offset:15360\n" \
    "ds_read_b64_tr_b16 v[128:129], v90 offset:20480\n" \
    "ds_read_b64_tr_b16 v[130:131], v90 offset:25600\n" \
    "ds_read_b64_tr_b16 v[132:133], v90 offset:30720\n" \
    "ds_read_b64_tr_b16 v[134:135], v90 offset:35840\n" \
    "ds_read_b64_tr_b16 v[120:121], v90 offset:32\n" \
    "ds_read_b64_tr_b16 v[122:123], v90 offset:5152\n" \
    "ds_read_b64_tr_b16 v[124:125], v90 offset:10272\n" \
    "ds_read_b64_tr_b16 v[126:127], v90 offset:15392\n" \
    "ds_read_b64_tr_b16 v[112:113], v90 offset:20512\n" \
    "ds_read_b64_tr_b16 v[114:115], v90 offset:25632\n" \
    "ds_read_b64_tr_b16 v[116:117], v90 offset:30752\n" \
    "ds_read_b64_tr_b16 v[118:119], v90 offset:35872\n" \
    "s_waitcnt lgkmcnt(0)\n" \
    "s_barrier\n" \
    "s_add_u32 s40, s12, 3145728\n" \
    "s_addc_u32 s41, s13, 0\n" \
    "s_add_u32 m0, s22, 65536\n" \
    "s_nop 0\n" \
    "global_load_lds_dwordx4 v84, s[40:41]\n" \
    "s_add_u32 m0, s22, 66560\n" \
    "s_nop 0\n" \
    "global_load_lds_dwordx4 v85, s[40:41]\n" \
    "s_add_u32 m0, s22, 67584\n" \
    "s_nop 0\n" \
    "global_load_lds_dwordx4 v86, s[40:41]\n" \
    "s_add_u32 m0, s22, 68608\n" \
    "s_nop 0\n" \
    "global_load_lds_dwordx4 v87, s[40:41]\n" \
    "s_add_u32 s40, s14, 3670016\n" \
    "s_addc_u32 s41, s15, 0\n" \
    "s_add_u32 m0, s22, 81920\n" \
    "s_nop 0\n" \
    "global_load_lds_dwordx4 v84, s[40:41]\n" \
    "s_add_u32 m0, s22, 82944\n" \
    "s_nop 0\n" \
    "global_load_lds_dwordx4 v85, s[40:41]\n" \
    "s_add_u32 m0, s22, 83968\n" \
    "s_nop 0\n" \
    "global_load_lds_dwordx4 v86, s[40:41]\n" \
    "s_add_u32 m0, s22, 84992\n" \
    "s_nop 0\n" \
    "global_load_lds_dwordx4 v87, s[40:41]\n" \
    "s_add_u32 s40, s14, 4194304\n" \
    "s_addc_u32 s41, s15, 0\n" \
    "s_add_u32 m0, s22, 98304\n" \
    "s_nop 0\n" \
    "global_load_lds_dwordx4 v84, s[40:41]\n" \
    "s_add_u32 m0, s22, 99328\n" \
    "s_nop 0\n" \
    "global_load_lds_dwordx4 v85, s[40:41]\n" \
    "s_add_u32 m0, s22, 100352\n" \
    "s_nop 0\n" \
    "global_load_lds_dwordx4 v86, s[40:41]\n" \
    "s_add_u32 m0, s22, 101376\n" \
    "s_nop 0\n" \
    "global_load_lds_dwordx4 v87, s[40:41]\n" \
    "s_waitcnt vmcnt(12)\n" \
    "s_waitcnt lgkmcnt(0)\n" \
    "s_barrier\n" \
    "ds_read_b64_tr_b16 v[16:17], v88\n" \
    "ds_read_b64_tr_b16 v[18:19], v88 offset:2048\n" \
    "ds_read_b64_tr_b16 v[20:21], v88 offset:8192\n" \
    "ds_read_b64_tr_b16 v[22:23], v88 offset:10240\n" \
    "ds_read_b64_tr_b16 v[24:25], v89\n" \
    "ds_read_b64_tr_b16 v[26:27], v89 offset:2048\n" \
    "ds_read_b64_tr_b16 v[28:29], v89 offset:8192\n" \
    "ds_read_b64_tr_b16 v[30:31], v89 offset:10240\n" \
    "ds_read_b64_tr_b16 v[32:33], v88 offset:16384\n" \
    "ds_read_b64_tr_b16 v[34:35], v88 offset:18432\n" \
    "ds_read_b64_tr_b16 v[36:37], v88 offset:24576\n" \
    "ds_read_b64_tr_b16 v[38:39], v88 offset:26624\n" \
    "ds_read_b64_tr_b16 v[40:41], v89 offset:16384\n" \
    "ds_read_b64_tr_b16 v[42:43], v89 offset:18432\n" \
    "ds_read_b64_tr_b16 v[44:45], v89 offset:24576\n" \
    "ds_read_b64_tr_b16 v[46:47], v89 offset:26624\n" \
    "ds_read_b64_tr_b16 v[48:49], v88 offset:32768\n" \
    "ds_read_b64_tr_b16 v[50:51], v88 offset:34816\n" \
    "ds_read_b64_tr_b16 v[52:53], v88 offset:40960\n" \
    "ds_read_b64_tr_b16 v[54:55], v88 offset:43008\n" \
    "ds_read_b64_tr_b16 v[56:57], v89 offset:32768\n" \
    "ds_read_b64_tr_b16 v[58:59], v89 offset:34816\n" \
    "ds_read_b64_tr_b16 v[60:61], v89 offset:40960\n" \
    "ds_read_b64_tr_b16 v[62:63], v89 offset:43008\n" \
    "ds_read_b64_tr_b16 v[64:65], v88 offset:49152\n" \
    "ds_read_b64_tr_b16 v[66:67], v88 offset:51200\n" \
    "ds_read_b64_tr_b16 v[68:69], v88 offset:57344\n" \
    "ds_read_b64_tr_b16 v[70:71], v88 offset:59392\n" \
    "ds_read_b64_tr_b16 v[72:73], v89 offset:49152\n" \
    "ds_read_b64_tr_b16 v[74:75], v89 offset:51200\n" \
    "ds_read_b64_tr_b16 v[76:77], v89 offset:57344\n" \
    "ds_read_b64_tr_b16 v[78:79], v89 offset:59392\n" \
    "s_waitcnt lgkmcnt(0)\n" \
    "s_barrier\n" \
    "ds_write_b128 v2, v[16:19]\n" \
    "ds_write_b128 v2, v[20:23] offset:1024\n" \
    "ds_write_b128 v2, v[24:27] offset:2048\n" \
    "ds_write_b128 v2, v[28:31] offset:3072\n" \
    "ds_write_b128 v2, v[32:35] offset:16384\n" \
    "ds_write_b128 v2, v[36:39] offset:17408\n" \
    "ds_write_b128 v2, v[40:43] offset:18432\n" \
    "ds_write_b128 v2, v[44:47] offset:19456\n" \
    "ds_write_b128 v2, v[48:51] offset:32768\n" \
    "ds_write_b128 v2, v[52:55] offset:33792\n" \
    "ds_write_b128 v2, v[56:59] offset:34816\n" \
    "ds_write_b128 v2, v[60:63] offset:35840\n" \
    "ds_write_b128 v2, v[64:67] offset:49152\n" \
    "ds_write_b128 v2, v[68:71] offset:50176\n" \
    "ds_write_b128 v2, v[72:75] offset:51200\n" \
    "ds_write_b128 v2, v[76:79] offset:52224\n" \
    "s_waitcnt vmcnt(0)\n" \
    "s_waitcnt lgkmcnt(0)\n" \
    "s_barrier\n" \
    "ds_read_b64_tr_b16 v[16:17], v92\n" \
    "ds_read_b64_tr_b16 v[18:19], v92 offset:2048\n" \
    "ds_read_b64_tr_b16 v[20:21], v92 offset:8192\n" \
    "ds_read_b64_tr_b16 v[22:23], v92 offset:10240\n" \
    "ds_read_b64_tr_b16 v[24:25], v93\n" \
    "ds_read_b64_tr_b16 v[26:27], v93 offset:2048\n" \
    "ds_read_b64_tr_b16 v[28:29], v93 offset:8192\n" \
    "ds_read_b64_tr_b16 v[30:31], v93 offset:10240\n" \
    "ds_read_b64_tr_b16 v[32:33], v92 offset:16384\n" \
    "ds_read_b64_tr_b16 v[34:35], v92 offset:18432\n" \
    "ds_read_b64_tr_b16 v[36:37], v92 offset:24576\n" \
    "ds_read_b64_tr_b16 v[38:39], v92 offset:26624\n" \
    "ds_read_b64_tr_b16 v[40:41], v93 offset:16384\n" \
    "ds_read_b64_tr_b16 v[42:43], v93 offset:18432\n" \
    "ds_read_b64_tr_b16 v[44:45], v93 offset:24576\n" \
    "ds_read_b64_tr_b16 v[46:47], v93 offset:26624\n" \
    "ds_read_b64_tr_b16 v[48:49], v92 offset:32768\n" \
    "ds_read_b64_tr_b16 v[50:51], v92 offset:34816\n" \
    "ds_read_b64_tr_b16 v[52:53], v92 offset:40960\n" \
    "ds_read_b64_tr_b16 v[54:55], v92 offset:43008\n" \
    "ds_read_b64_tr_b16 v[56:57], v93 offset:32768\n" \
    "ds_read_b64_tr_b16 v[58:59], v93 offset:34816\n" \
    "ds_read_b64_tr_b16 v[60:61], v93 offset:40960\n" \
    "ds_read_b64_tr_b16 v[62:63], v93 offset:43008\n" \
    "s_waitcnt lgkmcnt(0)\n" \
    "s_barrier\n" \
    "ds_write_b128 v3, v[16:19]\n" \
    "ds_write_b128 v3, v[20:23] offset:1024\n" \
    "ds_write_b128 v3, v[24:27] offset:2048\n" \
    "ds_write_b128 v3, v[28:31] offset:3072\n" \
    "ds_write_b128 v3, v[32:35] offset:16384\n" \
    "ds_write_b128 v3, v[36:39] offset:17408\n" \
    "ds_write_b128 v3, v[40:43] offset:18432\n" \
    "ds_write_b128 v3, v[44:47] offset:19456\n" \
    "ds_write_b128 v3, v[48:51] offset:32768\n" \
    "ds_write_b128 v3, v[52:55] offset:33792\n" \
    "ds_write_b128 v3, v[56:59] offset:34816\n" \
    "ds_write_b128 v3, v[60:63] offset:35840\n" \
    "s_waitcnt lgkmcnt(0)\n" \
    "s_barrier\n" \
    "ds_read_b128 v[16:19], v12\n" \
    "ds_read_b128 v[20:23], v12 offset:4096\n" \
    "ds_read_b128 v[24:27], v12 offset:1024\n" \
    "ds_read_b128 v[28:31], v12 offset:5120\n" \
    "ds_read_b128 v[32:35], v12 offset:2048\n" \
    "ds_read_b128 v[36:39], v12 offset:6144\n" \
    "ds_read_b128 v[40:43], v12 offset:3072\n" \
    "ds_read_b128 v[44:47], v12 offset:7168\n" \
    "v_xor_b32 v12, 0x2000, v12\n" \
    "s_waitcnt lgkmcnt(7)\n" \
    "v_mfma_f32_16x16x32_bf16 v[48:51], v[16:19], v[136:139], 0\n" \
    "ds_read_b128 v[76:79], v1 offset:40960\n" \
    "s_waitcnt lgkmcnt(7)\n" \
    "v_mfma_f32_16x16x32_bf16 v[52:55], v[20:23], v[136:139], 0\n" \
    "ds_read_b128 v[80:83], v1 offset:40960\n" \
    "v_mfma_f32_16x16x32_bf16 v[56:59], v[16:19], v[120:123], 0\n" \
    "v_mfma_f32_16x16x32_bf16 v[60:63], v[20:23], v[120:123], 0\n" \
    "s_waitcnt lgkmcnt(7)\n" \
    "v_mfma_f32_16x16x32_bf16 v[48:51], v[24:27], v[140:143], v[48:51]\n" \
    "s_waitcnt lgkmcnt(6)\n" \
    "v_mfma_f32_16x16x32_bf16 v[52:55], v[28:31], v[140:143], v[52:55]\n" \
    "v_mfma_f32_16x16x32_bf16 v[56:59], v[24:27], v[124:127], v[56:59]\n" \
    "v_mfma_f32_16x16x32_bf16 v[60:63], v[28:31], v[124:127], v[60:63]\n" \
    "s_waitcnt lgkmcnt(5)\n" \
    "v_mfma_f32_16x16x32_bf16 v[48:51], v[32:35], v[128:131], v[48:51]\n" \
    "ds_read_b128 v[16:19], v1 offset:40960\n" \
    "s_waitcnt lgkmcnt(5)\n" \
    "v_mfma_f32_16x16x32_bf16 v[52:55], v[36:39], v[128:131], v[52:55]\n" \
    "ds_read_b128 v[20:23], v1 offset:40960\n" \
    "v_mfma_f32_16x16x32_bf16 v[56:59], v[32:35], v[112:115], v[56:59]\n" \
    "ds_read_b128 v[24:27], v1 offset:40960\n" \
    "v_mfma_f32_16x16x32_bf16 v[60:63], v[36:39], v[112:115], v[60:63]\n" \
    "ds_read_b128 v[28:31], v1 offset:40960\n" \
    "s_waitcnt lgkmcnt(7)\n" \
    "v_mfma_f32_16x16x32_bf16 v[48:51], v[40:43], v[132:135], v[48:51]\n" \
    "s_waitcnt lgkmcnt(6)\n" \
    "v_mfma_f32_16x16x32_bf16 v[52:55], v[44:47], v[132:135], v[52:55]\n" \
    "v_mfma_f32_16x16x32_bf16 v[56:59], v[40:43], v[116:119], v[56:59]\n" \
    "v_mfma_f32_16x16x32_bf16 v[60:63], v[44:47], v[116:119], v[60:63]\n" \
    "v_mov_b32 v8, v8\n" \
    "v_mov_b32 v8, v8\n" \
    "v_mov_b32 v8, v8\n" \
    "v_mov_b32 v8, v8\n" \
    "v_cvt_pk_bf16_f32 v84, v48, v49\n" \
    "v_cvt_pk_bf16_f32 v85, v50, v51\n" \
    "v_cvt_pk_bf16_f32 v86, v52, v53\n" \
    "v_cvt_pk_bf16_f32 v87, v54, v55\n" \
    "v_cvt_pk_bf16_f32 v88, v56, v57\n" \
    "v_cvt_pk_bf16_f32 v89, v58, v59\n" \
    "v_cvt_pk_bf16_f32 v90, v60, v61\n" \
    "v_cvt_pk_bf16_f32 v91, v62, v63\n" \
    "ds_read_b128 v[48:51], v2\n" \
    "ds_read_b128 v[52:55], v2 offset:1024\n" \
    "ds_read_b128 v[56:59], v2 offset:2048\n" \
    "ds_read_b128 v[60:63], v2 offset:3072\n" \
    "ds_read_b128 v[64:67], v2 offset:16384\n" \
    "ds_read_b128 v[68:71], v2 offset:17408\n" \
    ".p2align 6\n" \
    "LOOP_2_%=:\n" \
    "s_waitcnt lgkmcnt(0)\n" \
    "s_barrier\n" \
    "ds_read_b128 v[32:35], v1\n" \
    "ds_read_b128 v[36:39], v1 offset:1024\n" \
    "ds_read_b128 v[40:43], v1 offset:2048\n" \
    "ds_read_b128 v[44:47], v1 offset:3072\n" \
    "s_waitcnt lgkmcnt(3)\n" \
    "v_smfmac_f32_16x16x64_bf16 v[16:19], v[32:35], v[48:55], v0\n" \
    "ds_read_b128 v[48:51], v2 offset:18432\n" \
    "ds_read_b128 v[52:55], v2 offset:19456\n" \
    "v_smfmac_f32_16x16x64_bf16 v[24:27], v[32:35], a[0:7], v0\n" \
    "v_smfmac_f32_16x16x64_bf16 v[28:31], v[32:35], a[8:15], v0\n" \
    "v_smfmac_f32_16x16x64_bf16 v[20:23], v[32:35], v[56:63], v0\n" \
    "ds_read_b128 v[32:35], v1 offset:4096\n" \
    "ds_read_b128 v[56:59], v2 offset:32768\n" \
    "ds_read_b128 v[60:63], v2 offset:33792\n" \
    "s_waitcnt lgkmcnt(7)\n" \
    "v_smfmac_f32_16x16x64_bf16 v[16:19], v[36:39], a[32:39], v0\n" \
    "v_smfmac_f32_16x16x64_bf16 v[24:27], v[36:39], a[48:55], v0\n" \
    "v_smfmac_f32_16x16x64_bf16 v[28:31], v[36:39], a[56:63], v0\n" \
    "v_smfmac_f32_16x16x64_bf16 v[20:23], v[36:39], a[40:47], v0\n" \
    "ds_read_b128 v[36:39], v1 offset:5120\n" \
    "s_waitcnt lgkmcnt(7)\n" \
    "v_smfmac_f32_16x16x64_bf16 v[24:27], v[40:43], v[64:71], v0\n" \
    "ds_read_b128 v[64:67], v2 offset:34816\n" \
    "ds_read_b128 v[68:71], v2 offset:35840\n" \
    "v_smfmac_f32_16x16x64_bf16 v[16:19], v[40:43], a[80:87], v0\n" \
    "v_smfmac_f32_16x16x64_bf16 v[20:23], v[40:43], a[88:95], v0\n" \
    "s_waitcnt lgkmcnt(6)\n" \
    "v_smfmac_f32_16x16x64_bf16 v[28:31], v[40:43], v[48:55], v0\n" \
    "ds_read_b128 v[40:43], v1 offset:6144\n" \
    "ds_read_b128 v[48:51], v2 offset:49152\n" \
    "ds_read_b128 v[52:55], v2 offset:50176\n" \
    "s_waitcnt lgkmcnt(6)\n" \
    "v_smfmac_f32_16x16x64_bf16 v[16:19], v[44:47], v[56:63], v0\n" \
    "ds_read_b128 v[56:59], v2 offset:51200\n" \
    "ds_read_b128 v[60:63], v2 offset:52224\n" \
    "v_smfmac_f32_16x16x64_bf16 v[24:27], v[44:47], a[112:119], v0\n" \
    "v_smfmac_f32_16x16x64_bf16 v[28:31], v[44:47], a[120:127], v0\n" \
    "s_waitcnt lgkmcnt(5)\n" \
    "v_smfmac_f32_16x16x64_bf16 v[20:23], v[44:47], v[64:71], v0\n" \
    "ds_read_b128 v[44:47], v1 offset:7168\n" \
    "ds_read_b128 v[64:67], v3\n" \
    "ds_read_b128 v[68:71], v3 offset:1024\n" \
    "s_waitcnt lgkmcnt(5)\n" \
    "v_smfmac_f32_16x16x64_bf16 v[24:27], v[32:35], v[48:55], v0\n" \
    "ds_read_b128 v[48:51], v3 offset:2048\n" \
    "ds_read_b128 v[52:55], v3 offset:3072\n" \
    "v_smfmac_f32_16x16x64_bf16 v[16:19], v[32:35], a[144:151], v0\n" \
    "v_smfmac_f32_16x16x64_bf16 v[20:23], v[32:35], a[152:159], v0\n" \
    "s_waitcnt lgkmcnt(5)\n" \
    "v_smfmac_f32_16x16x64_bf16 v[28:31], v[32:35], v[56:63], v0\n" \
    "ds_read_b128 v[32:35], v1 offset:8192\n" \
    "ds_read_b128 v[56:59], v3 offset:16384\n" \
    "ds_read_b128 v[60:63], v3 offset:17408\n" \
    "v_smfmac_f32_16x16x64_bf16 v[16:19], v[36:39], a[176:183], v0\n" \
    "v_smfmac_f32_16x16x64_bf16 v[24:27], v[36:39], a[192:199], v0\n" \
    "ds_read_b128 v[72:75], v1 offset:13312\n" \
    "v_smfmac_f32_16x16x64_bf16 v[28:31], v[36:39], a[200:207], v0\n" \
    "v_smfmac_f32_16x16x64_bf16 v[20:23], v[36:39], a[184:191], v0\n" \
    "ds_read_b128 v[36:39], v1 offset:9216\n" \
    "s_waitcnt lgkmcnt(7)\n" \
    "v_smfmac_f32_16x16x64_bf16 v[16:19], v[40:43], v[64:71], v0\n" \
    "ds_read_b128 v[64:67], v3 offset:18432\n" \
    "ds_read_b128 v[68:71], v3 offset:19456\n" \
    "v_smfmac_f32_16x16x64_bf16 v[24:27], v[40:43], a[224:231], v0\n" \
    "v_smfmac_f32_16x16x64_bf16 v[28:31], v[40:43], a[232:239], v0\n" \
    "s_waitcnt lgkmcnt(7)\n" \
    "v_smfmac_f32_16x16x64_bf16 v[20:23], v[40:43], v[48:55], v0\n" \
    "ds_read_b128 v[48:51], v3 offset:32768\n" \
    "ds_read_b128 v[52:55], v3 offset:33792\n" \
    "s_waitcnt lgkmcnt(6)\n" \
    "v_smfmac_f32_16x16x64_bf16 v[24:27], v[44:47], v[56:63], v0\n" \
    "ds_read_b128 v[56:59], v3 offset:34816\n" \
    "ds_read_b128 v[60:63], v3 offset:35840\n" \
    "v_smfmac_f32_16x16x64_bf16 v[16:19], v[44:47], v[248:255], v0\n" \
    "v_smfmac_f32_16x16x64_bf16 v[20:23], v[44:47], v[240:247], v0\n" \
    "s_waitcnt lgkmcnt(4)\n" \
    "v_smfmac_f32_16x16x64_bf16 v[28:31], v[44:47], v[64:71], v0\n" \
    "s_waitcnt lgkmcnt(2)\n" \
    "v_smfmac_f32_16x16x64_bf16 v[24:27], v[32:35], v[48:55], v0\n" \
    "v_smfmac_f32_16x16x64_bf16 v[16:19], v[32:35], v[216:223], v0\n" \
    "ds_read_b128 v[64:67], v1 offset:12288\n" \
    "v_smfmac_f32_16x16x64_bf16 v[16:19], v[36:39], v[184:191], v0\n" \
    "ds_read_b128 v[68:71], v1 offset:16384\n" \
    "ds_read_b128 v[48:51], v1 offset:10240\n" \
    "v_smfmac_f32_16x16x64_bf16 v[24:27], v[36:39], v[168:175], v0\n" \
    "ds_read_b128 v[52:55], v1 offset:14336\n" \
    "s_waitcnt lgkmcnt(4)\n" \
    "v_smfmac_f32_16x16x64_bf16 v[28:31], v[32:35], v[56:63], v0\n" \
    "v_smfmac_f32_16x16x64_bf16 v[20:23], v[32:35], v[208:215], v0\n" \
    "v_smfmac_f32_16x16x64_bf16 v[28:31], v[36:39], v[160:167], v0\n" \
    "ds_read_b128 v[56:59], v1 offset:11264\n" \
    "v_add_f32 v16, v16, v17\n" \
    "v_smfmac_f32_16x16x64_bf16 v[20:23], v[36:39], v[176:183], v0\n" \
    "ds_read_b128 v[60:63], v1 offset:15360\n" \
    "v_add_f32 v24, v24, v25\n" \
    "v_smfmac_f32_16x16x64_bf16 v[76:79], v[40:43], a[240:247], v0\n" \
    "v_add_f32 v18, v18, v19\n" \
    "v_smfmac_f32_16x16x64_bf16 v[80:83], v[40:43], a[248:255], v0\n" \
    "ds_read_b128 v[40:43], v1\n" \
    "v_add_f32 v26, v26, v27\n" \
    "v_smfmac_f32_16x16x64_bf16 v[76:79], v[44:47], v[232:239], v0\n" \
    "v_cvt_pk_bf16_f32 v16, v16, v18\n" \
    "v_add_f32 v28, v28, v29\n" \
    "v_smfmac_f32_16x16x64_bf16 v[80:83], v[44:47], v[224:231], v0\n" \
    "ds_read_b128 v[44:47], v1 offset:1024\n" \
    "v_cvt_pk_bf16_f32 v24, v24, v26\n" \
    "v_add_f32 v20, v20, v21\n" \
    "v_smfmac_f32_16x16x64_bf16 v[76:79], v[32:35], v[200:207], v0\n" \
    "v_add_f32 v30, v30, v31\n" \
    "v_add_f32 v22, v22, v23\n" \
    "v_smfmac_f32_16x16x64_bf16 v[80:83], v[32:35], v[192:199], v0\n" \
    "ds_read_b128 v[32:35], v1 offset:2048\n" \
    "v_cvt_pk_bf16_f32 v28, v28, v30\n" \
    "v_cvt_pk_bf16_f32 v20, v20, v22\n" \
    "v_smfmac_f32_16x16x64_bf16 v[76:79], v[36:39], v[152:159], v0\n" \
    "ds_write2st64_b32 v5, v16, v20 offset0:0 offset1:1\n" \
    "ds_write2st64_b32 v5, v24, v28 offset0:8 offset1:9\n" \
    "v_smfmac_f32_16x16x64_bf16 v[80:83], v[36:39], v[144:151], v0\n" \
    "ds_read_b128 v[36:39], v1 offset:3072\n" \
    "s_waitcnt lgkmcnt(5)\n" \
    "v_smfmac_f32_16x16x64_bf16 v[76:79], v[40:43], a[16:23], v0\n" \
    "v_smfmac_f32_16x16x64_bf16 v[80:83], v[40:43], a[24:31], v0\n" \
    "ds_read_b128 v[40:43], v1 offset:4096\n" \
    "s_waitcnt lgkmcnt(5)\n" \
    "v_smfmac_f32_16x16x64_bf16 v[76:79], v[44:47], a[64:71], v0\n" \
    "s_waitcnt lgkmcnt(2)\n" \
    "s_barrier\n" \
    "ds_read_b64_tr_b16 v[16:17], v4\n" \
    "ds_read_b64_tr_b16 v[18:19], v4 offset:256\n" \
    "v_smfmac_f32_16x16x64_bf16 v[80:83], v[44:47], a[72:79], v0\n" \
    "ds_read_b128 v[44:47], v1 offset:5120\n" \
    "ds_read_b64_tr_b16 v[20:21], v4 offset:512\n" \
    "ds_read_b64_tr_b16 v[22:23], v4 offset:768\n" \
    "v_smfmac_f32_16x16x64_bf16 v[76:79], v[32:35], a[96:103], v0\n" \
    "ds_read_b64_tr_b16 v[24:25], v4 offset:1024\n" \
    "ds_read_b64_tr_b16 v[26:27], v4 offset:1280\n" \
    "v_smfmac_f32_16x16x64_bf16 v[80:83], v[32:35], a[104:111], v0\n" \
    "ds_read_b128 v[32:35], v1 offset:17408\n" \
    "ds_read_b64_tr_b16 v[28:29], v4 offset:1536\n" \
    "ds_read_b64_tr_b16 v[30:31], v4 offset:1792\n" \
    "s_waitcnt lgkmcnt(11)\n" \
    "v_smfmac_f32_16x16x64_bf16 v[76:79], v[36:39], a[128:135], v0\n" \
    "v_smfmac_f32_16x16x64_bf16 v[80:83], v[36:39], a[136:143], v0\n" \
    "s_waitcnt lgkmcnt(10)\n" \
    "v_smfmac_f32_16x16x64_bf16 v[76:79], v[40:43], a[160:167], v0\n" \
    "v_smfmac_f32_16x16x64_bf16 v[80:83], v[40:43], a[168:175], v0\n" \
    "s_waitcnt lgkmcnt(7)\n" \
    "v_smfmac_f32_16x16x64_bf16 v[76:79], v[44:47], a[208:215], v0\n" \
    "v_smfmac_f32_16x16x64_bf16 v[80:83], v[44:47], a[216:223], v0\n" \
    "v_mfma_f32_16x16x32_bf16 v[36:39], v[48:51], v[16:19], 0\n" \
    "v_mfma_f32_16x16x32_bf16 v[40:43], v[52:55], v[16:19], 0\n" \
    "ds_read_b64_tr_b16 v[48:49], v4 offset:2048\n" \
    "ds_read_b64_tr_b16 v[50:51], v4 offset:2304\n" \
    "s_waitcnt lgkmcnt(7)\n" \
    "v_mfma_f32_16x16x32_bf16 v[36:39], v[56:59], v[20:23], v[36:39]\n" \
    "ds_read_b64_tr_b16 v[52:53], v4 offset:2560\n" \
    "ds_read_b64_tr_b16 v[54:55], v4 offset:2816\n" \
    "v_add_f32 v76, v76, v77\n" \
    "v_mfma_f32_16x16x32_bf16 v[40:43], v[60:63], v[20:23], v[40:43]\n" \
    "ds_read_b64_tr_b16 v[56:57], v4 offset:3072\n" \
    "ds_read_b64_tr_b16 v[58:59], v4 offset:3328\n" \
    "v_add_f32 v80, v80, v81\n" \
    "v_add_f32 v78, v78, v79\n" \
    "s_waitcnt lgkmcnt(9)\n" \
    "v_mfma_f32_16x16x32_bf16 v[36:39], v[64:67], v[24:27], v[36:39]\n" \
    "ds_read_b64_tr_b16 v[60:61], v4 offset:3584\n" \
    "ds_read_b64_tr_b16 v[62:63], v4 offset:3840\n" \
    "v_add_f32 v82, v82, v83\n" \
    "v_cvt_pk_bf16_f32 v76, v76, v78\n" \
    "v_mfma_f32_16x16x32_bf16 v[40:43], v[68:71], v[24:27], v[40:43]\n" \
    "v_cvt_pk_bf16_f32 v80, v80, v82\n" \
    "ds_write2st64_b32 v5, v76, v80 offset0:16 offset1:17\n" \
    "s_waitcnt lgkmcnt(9)\n" \
    "v_mfma_f32_16x16x32_bf16 v[36:39], v[72:75], v[28:31], v[36:39]\n" \
    "ds_read_b128 v[80:83], v1 offset:40960\n" \
    "v_mfma_f32_16x16x32_bf16 v[40:43], v[32:35], v[28:31], v[40:43]\n" \
    "ds_read_b128 v[76:79], v1 offset:40960\n" \
    "s_waitcnt lgkmcnt(9)\n" \
    "v_mfma_f32_16x16x32_bf16 v[44:47], v[48:51], v[16:19], 0\n" \
    "s_waitcnt lgkmcnt(2)\n" \
    "s_barrier\n" \
    "ds_read_b64_tr_b16 v[64:65], v7\n" \
    "ds_read_b64_tr_b16 v[66:67], v7 offset:64\n" \
    "ds_read_b64_tr_b16 v[68:69], v7 offset:1024\n" \
    "ds_read_b64_tr_b16 v[70:71], v7 offset:1088\n" \
    "v_mfma_f32_16x16x32_bf16 v[44:47], v[52:55], v[20:23], v[44:47]\n" \
    "s_cmp_eq_u32 s28, 63\n" \
    "s_cbranch_scc1 LAST_2_%=\n" \
    "v_mfma_f32_16x16x32_bf16 v[44:47], v[56:59], v[24:27], v[44:47]\n" \
    "v_max3_f32 v53, v36, v37, v38\n" \
    "v_max3_f32 v53, v53, v39, v40\n" \
    "v_mfma_f32_16x16x32_bf16 v[44:47], v[60:63], v[28:31], v[44:47]\n" \
    "v_max3_f32 v53, v53, v41, v42\n" \
    "v_max_f32 v53, v53, v43\n" \
    "ds_read_b128 v[28:31], v1 offset:41984\n" \
    "ds_read_b64 v[56:57], v1 offset:43008\n" \
    "v_mov_b32 v55, v53\n" \
    "s_waitcnt lgkmcnt(4)\n" \
    "v_smfmac_f32_16x16x64_bf16 v[76:79], v[64:67], v[136:143], v6\n" \
    "v_smfmac_f32_16x16x64_bf16 v[80:83], v[64:67], v[120:127], v6\n" \
    "v_permlane32_swap_b32 v53, v55\n" \
    "v_max_f32 v53, v53, v55\n" \
    "v_max3_f32 v54, v44, v45, v46\n" \
    "v_max3_f32 v54, v54, v47, v53\n" \
    "v_mov_b32 v55, v54\n" \
    "s_waitcnt lgkmcnt(2)\n" \
    "v_smfmac_f32_16x16x64_bf16 v[76:79], v[68:71], v[128:135], v6\n" \
    "v_smfmac_f32_16x16x64_bf16 v[80:83], v[68:71], v[112:119], v6\n" \
    "v_permlane16_swap_b32 v54, v55\n" \
    "v_max_f32 v54, v54, v55\n" \
    "v_mul_f32 v54, s27, v54\n" \
    "v_fma_f32 v36, v36, s27, -v54\n" \
    "v_fma_f32 v37, v37, s27, -v54\n" \
    "v_fma_f32 v38, v38, s27, -v54\n" \
    "v_fma_f32 v39, v39, s27, -v54\n" \
    "v_exp_f32 v36, v36\n" \
    "v_exp_f32 v37, v37\n" \
    "v_exp_f32 v38, v38\n" \
    "v_exp_f32 v39, v39\n" \
    "v_fma_f32 v40, v40, s27, -v54\n" \
    "v_fma_f32 v41, v41, s27, -v54\n" \
    "v_fma_f32 v42, v42, s27, -v54\n" \
    "v_fma_f32 v43, v43, s27, -v54\n" \
    "v_exp_f32 v40, v40\n" \
    "v_exp_f32 v41, v41\n" \
    "v_exp_f32 v42, v42\n" \
    "v_exp_f32 v43, v43\n" \
    "v_fma_f32 v44, v44, s27, -v54\n" \
    "v_cvt_pk_bf16_f32 v48, v36, v37\n" \
    "v_cvt_pk_bf16_f32 v49, v38, v39\n" \
    "v_cvt_pk_bf16_f32 v50, v40, v41\n" \
    "v_cvt_pk_bf16_f32 v51, v42, v43\n" \
    "v_fma_f32 v45, v45, s27, -v54\n" \
    "v_fma_f32 v46, v46, s27, -v54\n" \
    "s_waitcnt lgkmcnt(1)\n" \
    "v_mfma_f32_16x16x32_bf16 v[60:63], v[28:31], v[48:51], 0\n" \
    "v_fma_f32 v47, v47, s27, -v54\n" \
    "v_exp_f32 v44, v44\n" \
    "v_mfma_f32_16x16x32_bf16 v[16:19], v[84:87], v[48:51], 0\n" \
    "v_exp_f32 v45, v45\n" \
    "v_exp_f32 v46, v46\n" \
    "v_mfma_f32_16x16x32_bf16 v[20:23], v[88:91], v[48:51], 0\n" \
    "v_exp_f32 v47, v47\n" \
    "v_cvt_pk_bf16_f32 v52, v44, v45\n" \
    "v_cvt_pk_bf16_f32 v53, v46, v47\n" \
    "v_cvt_pk_bf16_f32 v76, v76, v77\n" \
    "v_cvt_pk_bf16_f32 v77, v78, v79\n" \
    "s_waitcnt lgkmcnt(0)\n" \
    "v_mfma_f32_16x16x16_bf16 v[60:63], v[56:57], v[52:53], v[60:63]\n" \
    "v_cvt_pk_bf16_f32 v80, v80, v81\n" \
    "v_cvt_pk_bf16_f32 v81, v82, v83\n" \
    "v_mfma_f32_16x16x16_bf16 v[16:19], v[76:77], v[52:53], v[16:19]\n" \
    "v_mov_b32 v8, v8\n" \
    "v_mfma_f32_16x16x16_bf16 v[20:23], v[80:81], v[52:53], v[20:23]\n" \
    "v_mov_b32 v8, v8\n" \
    "v_mov_b32 v8, v8\n" \
    "v_mov_b32 v8, v8\n" \
    "v_rcp_f32 v54, v60\n" \
    "v_mov_b32 v8, v8\n" \
    "v_mov_b32 v8, v8\n" \
    "v_pk_mul_f32 v[16:17], v[16:17], v[54:55] op_sel_hi:[1,0]\n" \
    "v_pk_mul_f32 v[18:19], v[18:19], v[54:55] op_sel_hi:[1,0]\n" \
    "v_pk_mul_f32 v[20:21], v[20:21], v[54:55] op_sel_hi:[1,0]\n" \
    "v_pk_mul_f32 v[22:23], v[22:23], v[54:55] op_sel_hi:[1,0]\n" \
    "v_cvt_pk_bf16_f32 v16, v16, v17\n" \
    "v_cvt_pk_bf16_f32 v17, v18, v19\n" \
    "v_cvt_pk_bf16_f32 v18, v20, v21\n" \
    "v_cvt_pk_bf16_f32 v19, v22, v23\n" \
    "v_mov_b32 v8, v8\n" \
    "v_permlane16_swap_b32 v16, v18\n" \
    "v_permlane16_swap_b32 v17, v19\n" \
    "v_bfi_b32 v16, 1, s35, v16\n" \
    "v_bfi_b32 v18, 1, s35, v18\n" \
    "v_bfi_b32 v17, 1, s35, v17\n" \
    "v_bfi_b32 v19, 1, s35, v19\n" \
    "s_mov_b64 exec, s[44:45]\n" \
    "buffer_store_dwordx4 v[16:19], v9, s[8:11], 0 offen sc1\n" \
    "s_mov_b64 exec, -1\n" \
    "v_mov_b32 v8, v8\n" \
    "ds_read_b128 v[16:19], v12\n" \
    "ds_read_b128 v[20:23], v12 offset:4096\n" \
    "ds_read_b128 v[24:27], v12 offset:1024\n" \
    "ds_read_b128 v[28:31], v12 offset:5120\n" \
    "ds_read_b128 v[32:35], v12 offset:2048\n" \
    "ds_read_b128 v[36:39], v12 offset:6144\n" \
    "ds_read_b128 v[40:43], v12 offset:3072\n" \
    "ds_read_b128 v[44:47], v12 offset:7168\n" \
    "v_xor_b32 v12, 0x2000, v12\n" \
    "s_waitcnt lgkmcnt(7)\n" \
    "v_mfma_f32_16x16x32_bf16 v[48:51], v[16:19], v[136:139], 0\n" \
    "s_waitcnt lgkmcnt(6)\n" \
    "v_mfma_f32_16x16x32_bf16 v[52:55], v[20:23], v[136:139], 0\n" \
    "v_mfma_f32_16x16x32_bf16 v[56:59], v[16:19], v[120:123], 0\n" \
    "v_mfma_f32_16x16x32_bf16 v[60:63], v[20:23], v[120:123], 0\n" \
    "s_waitcnt lgkmcnt(5)\n" \
    "v_mfma_f32_16x16x32_bf16 v[48:51], v[24:27], v[140:143], v[48:51]\n" \
    "s_waitcnt lgkmcnt(4)\n" \
    "v_mfma_f32_16x16x32_bf16 v[52:55], v[28:31], v[140:143], v[52:55]\n" \
    "v_mfma_f32_16x16x32_bf16 v[56:59], v[24:27], v[124:127], v[56:59]\n" \
    "v_mfma_f32_16x16x32_bf16 v[60:63], v[28:31], v[124:127], v[60:63]\n" \
    "s_waitcnt lgkmcnt(3)\n" \
    "v_mfma_f32_16x16x32_bf16 v[48:51], v[32:35], v[128:131], v[48:51]\n" \
    "ds_read_b128 v[16:19], v1 offset:40960\n" \
    "s_waitcnt lgkmcnt(3)\n" \
    "v_mfma_f32_16x16x32_bf16 v[52:55], v[36:39], v[128:131], v[52:55]\n" \
    "ds_read_b128 v[20:23], v1 offset:40960\n" \
    "v_mfma_f32_16x16x32_bf16 v[56:59], v[32:35], v[112:115], v[56:59]\n" \
    "ds_read_b128 v[24:27], v1 offset:40960\n" \
    "v_mfma_f32_16x16x32_bf16 v[60:63], v[36:39], v[112:115], v[60:63]\n" \
    "ds_read_b128 v[28:31], v1 offset:40960\n" \
    "s_waitcnt lgkmcnt(5)\n" \
    "v_mfma_f32_16x16x32_bf16 v[48:51], v[40:43], v[132:135], v[48:51]\n" \
    "s_waitcnt lgkmcnt(4)\n" \
    "v_mfma_f32_16x16x32_bf16 v[52:55], v[44:47], v[132:135], v[52:55]\n" \
    "v_mfma_f32_16x16x32_bf16 v[56:59], v[40:43], v[116:119], v[56:59]\n" \
    "v_mfma_f32_16x16x32_bf16 v[60:63], v[44:47], v[116:119], v[60:63]\n" \
    "s_bitcmp1_b32 s26, 0\n" \
    "s_cbranch_scc0 NORL_2_%=\n" \
    "s_bfe_u32 s40, s26, 0x80008\n" \
    "s_cbranch_scc0 DL1_2_%=X\n" \
    ".p2align 5\n" \
    "DL1_2_%=:\n" \
    "s_nop 0\n" \
    "s_nop 0\n" \
    "s_sub_u32 s40, s40, 1\n" \
    "s_nop 0\n" \
    "s_cmp_gt_u32 s40, 0\n" \
    "s_nop 0\n" \
    "s_cbranch_scc1 DL1_2_%=\n" \
    "DL1_2_%=X:\n" \
    "buffer_load_dwordx4 v[72:75], v13, s[8:11], 0 offen sc1\n" \
    "buffer_load_dwordx4 v[76:79], v13, s[8:11], 0 offen offset:1024 sc1\n" \
    "buffer_load_dwordx4 v[80:83], v13, s[8:11], 0 offen offset:2048 sc1\n" \
    "buffer_load_dwordx4 v[92:95], v13, s[8:11], 0 offen offset:3072 sc1\n" \
    "NORL_2_%=:\n" \
    "v_cvt_pk_bf16_f32 v84, v48, v49\n" \
    "v_cvt_pk_bf16_f32 v85, v50, v51\n" \
    "v_cvt_pk_bf16_f32 v86, v52, v53\n" \
    "v_cvt_pk_bf16_f32 v87, v54, v55\n" \
    "v_cvt_pk_bf16_f32 v88, v56, v57\n" \
    "v_cvt_pk_bf16_f32 v89, v58, v59\n" \
    "v_cvt_pk_bf16_f32 v90, v60, v61\n" \
    "v_cvt_pk_bf16_f32 v91, v62, v63\n" \
    "ds_read_b128 v[48:51], v2\n" \
    "ds_read_b128 v[52:55], v2 offset:1024\n" \
    "ds_read_b128 v[56:59], v2 offset:2048\n" \
    "ds_read_b128 v[60:63], v2 offset:3072\n" \
    "ds_read_b128 v[64:67], v2 offset:16384\n" \
    "ds_read_b128 v[68:71], v2 offset:17408\n" \
    "s_bitcmp1_b32 s26, 0\n" \
    "s_cbranch_scc0 NORED_2_%=\n" \
    "ds_read_b128 v[40:43], v1 offset:44032\n" \
    "s_mov_b32 s40, 5\n" \
    ".p2align 5\n" \
    "DL2_2_%=:\n" \
    "s_nop 0\n" \
    "s_nop 0\n" \
    "s_sub_u32 s40, s40, 1\n" \
    "s_nop 0\n" \
    "s_cmp_gt_u32 s40, 0\n" \
    "s_nop 0\n" \
    "s_cbranch_scc1 DL2_2_%=\n" \
    "DL2_2_%=X:\n" \
    "s_branch REDL1_2_%=\n" \
    "REDR0_2_%=:\n" \
    "s_add_u32 s39, s39, 1\n" \
    "s_cmp_lt_u32 s39, 1048576\n" \
    "s_cbranch_scc0 FAIL_2_%=\n" \
    "buffer_load_dwordx4 v[72:75], v13, s[8:11], 0 offen sc1\n" \
    "buffer_load_dwordx4 v[76:79], v13, s[8:11], 0 offen offset:1024 sc1\n" \
    "buffer_load_dwordx4 v[80:83], v13, s[8:11], 0 offen offset:2048 sc1\n" \
    "buffer_load_dwordx4 v[92:95], v13, s[8:11], 0 offen offset:3072 sc1\n" \
    "s_waitcnt vmcnt(7)\n" \
    "s_waitcnt lgkmcnt(0)\n" \
    "v_mfma_f32_16x16x32_bf16 v[36:39], v[40:43], v[96:99], 0\n" \
    "v_bitop3_b32 v32, v96, v97, s36 bitop3:0x7e\n" \
    "v_bitop3_b32 v35, v98, v99, s36 bitop3:0x7e\n" \
    "s_waitcnt vmcnt(6)\n" \
    "v_mfma_f32_16x16x32_bf16 v[36:39], v[40:43], v[100:103], v[36:39]\n" \
    "v_bitop3_b32 v33, v100, v101, s36 bitop3:0x7e\n" \
    "v_bitop3_b32 v34, v102, v103, s36 bitop3:0x7e\n" \
    "v_or3_b32 v32, v32, v33, v34\n" \
    "s_waitcnt vmcnt(5)\n" \
    "v_mfma_f32_16x16x32_bf16 v[36:39], v[40:43], v[104:107], v[36:39]\n" \
    "v_bitop3_b32 v33, v104, v105, s36 bitop3:0x7e\n" \
    "v_bitop3_b32 v34, v106, v107, s36 bitop3:0x7e\n" \
    "v_or3_b32 v32, v32, v33, v34\n" \
    "s_waitcnt vmcnt(4)\n" \
    "v_mfma_f32_16x16x32_bf16 v[36:39], v[40:43], v[108:111], v[36:39]\n" \
    "v_bitop3_b32 v33, v108, v109, s36 bitop3:0x7e\n" \
    "v_bitop3_b32 v34, v110, v111, s36 bitop3:0x7e\n" \
    "v_or3_b32 v32, v32, v33, v34\n" \
    "v_or_b32 v32, v32, v35\n" \
    "v_and_b32 v32, 1, v32\n" \
    "v_cmp_ne_u32 vcc, 0, v32\n" \
    "v_mov_b32 v8, v8\n" \
    "v_mov_b32 v8, v8\n" \
    "v_add_f32_dpp v36, v36, v36 row_ror:8 row_mask:0xf bank_mask:0xf\n" \
    "v_add_f32_dpp v37, v37, v37 row_ror:8 row_mask:0xf bank_mask:0xf\n" \
    "v_add_f32_dpp v38, v38, v38 row_ror:8 row_mask:0xf bank_mask:0xf\n" \
    "v_add_f32_dpp v39, v39, v39 row_ror:8 row_mask:0xf bank_mask:0xf\n" \
    "s_cbranch_vccnz REDR1_2_%=\n" \
    "v_cvt_pk_bf16_f32 v36, v36, v37\n" \
    "v_cvt_pk_bf16_f32 v37, v38, v39\n" \
    "v_min_u32 v36, -2, v36\n" \
    "v_min_u32 v37, -2, v37\n" \
    "buffer_store_dwordx2 v[36:37], v14, s[8:11], s31 offen sc1\n" \
    "s_branch REDX_2_%=\n" \
    "REDR1_2_%=:\n" \
    "s_add_u32 s39, s39, 1\n" \
    "s_cmp_lt_u32 s39, 1048576\n" \
    "s_cbranch_scc0 FAIL_2_%=\n" \
    "REDL1_2_%=:\n" \
    "buffer_load_dwordx4 v[96:99], v13, s[8:11], 0 offen sc1\n" \
    "buffer_load_dwordx4 v[100:103], v13, s[8:11], 0 offen offset:1024 sc1\n" \
    "buffer_load_dwordx4 v[104:107], v13, s[8:11], 0 offen offset:2048 sc1\n" \
    "buffer_load_dwordx4 v[108:111], v13, s[8:11], 0 offen offset:3072 sc1\n" \
    "s_waitcnt vmcnt(7)\n" \
    "v_mfma_f32_16x16x32_bf16 v[36:39], v[40:43], v[72:75], 0\n" \
    "v_bitop3_b32 v32, v72, v73, s36 bitop3:0x7e\n" \
    "v_bitop3_b32 v35, v74, v75, s36 bitop3:0x7e\n" \
    "s_waitcnt vmcnt(6)\n" \
    "v_mfma_f32_16x16x32_bf16 v[36:39], v[40:43], v[76:79], v[36:39]\n" \
    "v_bitop3_b32 v33, v76, v77, s36 bitop3:0x7e\n" \
    "v_bitop3_b32 v34, v78, v79, s36 bitop3:0x7e\n" \
    "v_or3_b32 v32, v32, v33, v34\n" \
    "s_waitcnt vmcnt(5)\n" \
    "v_mfma_f32_16x16x32_bf16 v[36:39], v[40:43], v[80:83], v[36:39]\n" \
    "v_bitop3_b32 v33, v80, v81, s36 bitop3:0x7e\n" \
    "v_bitop3_b32 v34, v82, v83, s36 bitop3:0x7e\n" \
    "v_or3_b32 v32, v32, v33, v34\n" \
    "s_waitcnt vmcnt(4)\n" \
    "v_mfma_f32_16x16x32_bf16 v[36:39], v[40:43], v[92:95], v[36:39]\n" \
    "v_bitop3_b32 v33, v92, v93, s36 bitop3:0x7e\n" \
    "v_bitop3_b32 v34, v94, v95, s36 bitop3:0x7e\n" \
    "v_or3_b32 v32, v32, v33, v34\n" \
    "v_or_b32 v32, v32, v35\n" \
    "v_and_b32 v32, 1, v32\n" \
    "v_cmp_ne_u32 vcc, 0, v32\n" \
    "v_mov_b32 v8, v8\n" \
    "v_mov_b32 v8, v8\n" \
    "v_add_f32_dpp v36, v36, v36 row_ror:8 row_mask:0xf bank_mask:0xf\n" \
    "v_add_f32_dpp v37, v37, v37 row_ror:8 row_mask:0xf bank_mask:0xf\n" \
    "v_add_f32_dpp v38, v38, v38 row_ror:8 row_mask:0xf bank_mask:0xf\n" \
    "v_add_f32_dpp v39, v39, v39 row_ror:8 row_mask:0xf bank_mask:0xf\n" \
    "s_cbranch_vccnz REDR0_2_%=\n" \
    "v_cvt_pk_bf16_f32 v36, v36, v37\n" \
    "v_cvt_pk_bf16_f32 v37, v38, v39\n" \
    "v_min_u32 v36, -2, v36\n" \
    "v_min_u32 v37, -2, v37\n" \
    "buffer_store_dwordx2 v[36:37], v14, s[8:11], s31 offen sc1\n" \
    "REDX_2_%=:\n" \
    "s_bfe_u32 s40, s26, 0x80010\n" \
    "s_cbranch_scc0 DL3_2_%=X\n" \
    ".p2align 5\n" \
    "DL3_2_%=:\n" \
    "s_nop 0\n" \
    "s_nop 0\n" \
    "s_sub_u32 s40, s40, 1\n" \
    "s_nop 0\n" \
    "s_cmp_gt_u32 s40, 0\n" \
    "s_nop 0\n" \
    "s_cbranch_scc1 DL3_2_%=\n" \
    "DL3_2_%=X:\n" \
    "s_branch REDZ_2_%=\n" \
    "NORED_2_%=:\n" \
    "s_bfe_u32 s40, s26, 0x80018\n" \
    "s_cbranch_scc0 DL4_2_%=X\n" \
    ".p2align 5\n" \
    "DL4_2_%=:\n" \
    "s_nop 0\n" \
    "s_nop 0\n" \
    "s_sub_u32 s40, s40, 1\n" \
    "s_nop 0\n" \
    "s_cmp_gt_u32 s40, 0\n" \
    "s_nop 0\n" \
    "s_cbranch_scc1 DL4_2_%=\n" \
    "DL4_2_%=X:\n" \
    "REDZ_2_%=:\n" \
    "buffer_load_dwordx4 v[96:99], v10, s[8:11], s31 offen sc1\n" \
    "buffer_load_dwordx4 v[100:103], v10, s[8:11], s31 offen offset:1024 sc1\n" \
    "s_mov_b32 s40, 6\n" \
    ".p2align 5\n" \
    "DL5_2_%=:\n" \
    "s_nop 0\n" \
    "s_nop 0\n" \
    "s_sub_u32 s40, s40, 1\n" \
    "s_nop 0\n" \
    "s_cmp_gt_u32 s40, 0\n" \
    "s_nop 0\n" \
    "s_cbranch_scc1 DL5_2_%=\n" \
    "DL5_2_%=X:\n" \
    "s_branch GATL1_2_%=\n" \
    "GATR0_2_%=:\n" \
    "s_add_u32 s39, s39, 1\n" \
    "s_cmp_lt_u32 s39, 1048576\n" \
    "s_cbranch_scc0 FAIL_2_%=\n" \
    "buffer_load_dwordx4 v[96:99], v10, s[8:11], s31 offen sc1\n" \
    "buffer_load_dwordx4 v[100:103], v10, s[8:11], s31 offen offset:1024 sc1\n" \
    "s_waitcnt vmcnt(3)\n" \
    "ds_write_b128 v11, v[104:107]\n" \
    "v_max3_u32 v44, v104, v105, v106\n" \
    "v_max_u32 v44, v44, v107\n" \
    "s_waitcnt vmcnt(2)\n" \
    "ds_write_b128 v11, v[108:111] offset:1024\n" \
    "v_max3_u32 v44, v44, v108, v109\n" \
    "v_max3_u32 v44, v44, v110, v111\n" \
    "v_cmp_eq_u32 vcc, -1, v44\n" \
    "s_cbranch_vccnz GATR1_2_%=\n" \
    "s_branch GATX_2_%=\n" \
    "GATR1_2_%=:\n" \
    "s_add_u32 s39, s39, 1\n" \
    "s_cmp_lt_u32 s39, 1048576\n" \
    "s_cbranch_scc0 FAIL_2_%=\n" \
    "GATL1_2_%=:\n" \
    "buffer_load_dwordx4 v[104:107], v10, s[8:11], s31 offen sc1\n" \
    "buffer_load_dwordx4 v[108:111], v10, s[8:11], s31 offen offset:1024 sc1\n" \
    "s_waitcnt vmcnt(3)\n" \
    "ds_write_b128 v11, v[96:99]\n" \
    "v_max3_u32 v44, v96, v97, v98\n" \
    "v_max_u32 v44, v44, v99\n" \
    "s_waitcnt vmcnt(2)\n" \
    "ds_write_b128 v11, v[100:103] offset:1024\n" \
    "v_max3_u32 v44, v44, v100, v101\n" \
    "v_max3_u32 v44, v44, v102, v103\n" \
    "v_cmp_eq_u32 vcc, -1, v44\n" \
    "s_cbranch_vccnz GATR0_2_%=\n" \
    "GATX_2_%=:\n" \
    "ds_read_b128 v[76:79], v1 offset:40960\n" \
    "ds_read_b128 v[80:83], v1 offset:40960\n" \
    "s_add_u32 s28, s28, 1\n" \
    "s_add_u32 s31, s31, 32768\n" \
    "s_xor_b32 s35, s35, 1\n" \
    "s_not_b32 s36, s36\n" \
    "s_branch LOOP_2_%=\n" \
    "FAIL_2_%=:\n" \
    "v_mov_b32 v72, 1\n" \
    "s_mov_b32 s40, 2560\n" \
    "buffer_store_dword v72, off, s[8:11], s40 sc1\n" \
    "s_branch END_2_%=\n" \
    "LAST_2_%=:\n" \
    "v_mov_b32 v8, v8\n" \
    "v_mov_b32 v8, v8\n" \
    "v_mov_b32 v8, v8\n" \
    "v_mov_b32 v8, v8\n" \
    "v_mov_b32 v8, v8\n" \
    "v_mov_b32 v8, v8\n" \
    "v_mov_b32 v8, v8\n" \
    "v_mov_b32 v8, v8\n" \
    "s_waitcnt lgkmcnt(0)\n" \
    "END_2_%=:\n" \
    "s_waitcnt vmcnt(0) lgkmcnt(0)\n" \
    "s_endpgm\n" \
    ""
#define CHAIN_CLOBBERS "v26", "v27", "v28", "v29", "v30", "v31", "v32", "v33", "v34", "v35", "v36", "v37", "v38", "v39", "v40", "v41", "v42", "v43", "v44", "v45", "v46", "v47", "v48", "v49", "v50", "v51", "v52", "v53", "v54", "v55", "v56", "v57", "v58", "v59", "v60", "v61", "v62", "v63", "v64", "v65", "v66", "v67", "v68", "v69", "v70", "v71", "v72", "v73", "v74", "v75", "v76", "v77", "v78", "v79", "v80", "v81", "v82", "v83", "v92", "v93", "v94", "v95", "v96", "v97", "v98", "v99", "v100", "v101", "v102", "v103", "v104", "v105", "v106", "v107", "v108", "v109", "v110", "v111", "v112", "v113", "v114", "v115", "v116", "v117", "v118", "v119", "v120", "v121", "v122", "v123", "v124", "v125", "v126", "v127", "v128", "v129", "v130", "v131", "v132", "v133", "v134", "v135", "v136", "v137", "v138", "v139", "v140", "v141", "v142", "v143", "v144", "v145", "v146", "v147", "v148", "v149", "v150", "v151", "v152", "v153", "v154", "v155", "v156", "v157", "v158", "v159", "v160", "v161", "v162", "v163", "v164", "v165", "v166", "v167", "v168", "v169", "v170", "v171", "v172", "v173", "v174", "v175", "v176", "v177", "v178", "v179", "v180", "v181", "v182", "v183", "v184", "v185", "v186", "v187", "v188", "v189", "v190", "v191", "v192", "v193", "v194", "v195", "v196", "v197", "v198", "v199", "v200", "v201", "v202", "v203", "v204", "v205", "v206", "v207", "v208", "v209", "v210", "v211", "v212", "v213", "v214", "v215", "v216", "v217", "v218", "v219", "v220", "v221", "v222", "v223", "v224", "v225", "v226", "v227", "v228", "v229", "v230", "v231", "v232", "v233", "v234", "v235", "v236", "v237", "v238", "v239", "v240", "v241", "v242", "v243", "v244", "v245", "v246", "v247", "v248", "v249", "v250", "v251", "v252", "v253", "v254", "v255", "a0", "a1", "a2", "a3", "a4", "a5", "a6", "a7", "a8", "a9", "a10", "a11", "a12", "a13", "a14", "a15", "a16", "a17", "a18", "a19", "a20", "a21", "a22", "a23", "a24", "a25", "a26", "a27", "a28", "a29", "a30", "a31", "a32", "a33", "a34", "a35", "a36", "a37", "a38", "a39", "a40", "a41", "a42", "a43", "a44", "a45", "a46", "a47", "a48", "a49", "a50", "a51", "a52", "a53", "a54", "a55", "a56", "a57", "a58", "a59", "a60", "a61", "a62", "a63", "a64", "a65", "a66", "a67", "a68", "a69", "a70", "a71", "a72", "a73", "a74", "a75", "a76", "a77", "a78", "a79", "a80", "a81", "a82", "a83", "a84", "a85", "a86", "a87", "a88", "a89", "a90", "a91", "a92", "a93", "a94", "a95", "a96", "a97", "a98", "a99", "a100", "a101", "a102", "a103", "a104", "a105", "a106", "a107", "a108", "a109", "a110", "a111", "a112", "a113", "a114", "a115", "a116", "a117", "a118", "a119", "a120", "a121", "a122", "a123", "a124", "a125", "a126", "a127", "a128", "a129", "a130", "a131", "a132", "a133", "a134", "a135", "a136", "a137", "a138", "a139", "a140", "a141", "a142", "a143", "a144", "a145", "a146", "a147", "a148", "a149", "a150", "a151", "a152", "a153", "a154", "a155", "a156", "a157", "a158", "a159", "a160", "a161", "a162", "a163", "a164", "a165", "a166", "a167", "a168", "a169", "a170", "a171", "a172", "a173", "a174", "a175", "a176", "a177", "a178", "a179", "a180", "a181", "a182", "a183", "a184", "a185", "a186", "a187", "a188", "a189", "a190", "a191", "a192", "a193", "a194", "a195", "a196", "a197", "a198", "a199", "a200", "a201", "a202", "a203", "a204", "a205", "a206", "a207", "a208", "a209", "a210", "a211", "a212", "a213", "a214", "a215", "a216", "a217", "a218", "a219", "a220", "a221", "a222", "a223", "a224", "a225", "a226", "a227", "a228", "a229", "a230", "a231", "a232", "a233", "a234", "a235", "a236", "a237", "a238", "a239", "a240", "a241", "a242", "a243", "a244", "a245", "a246", "a247", "a248", "a249", "a250", "a251", "a252", "a253", "a254", "a255", "s27", "s28", "s29", "s30", "s31", "s34", "s35", "s36", "s37", "s38", "s39", "s40", "s41", "s42", "s43", "s44", "s45", "s46", "s68", "s69", "s70", "s71", "s72", "s73", "s74", "s75", "s76", "s77", "s78", "s79", "s80", "s81", "s82", "s83", "vcc", "scc", "memory"
#define CHAIN_STAMP 0
typedef unsigned long long u64;
DEV void chain_wave(const Args& a, const int role, const int w, const int l) {
    const u32 fb_ = WS_CTL;
    const int h = role >> 2, c = role & 3, NT = w < 2 ? 3 : 2;
    const int i16 = l & 15, g = l >> 4, q = (l >> 2) & 3, p = l & 3;
    const u32 selA = 0, selB = 0, l16 = l * 16;
    const int t0 = NT == 3 ? 3 * w : 2 * w + 2;
    const int gb0 = NT == 3 ? 3 * w : 2 * w + 2;
    u32 V[26], X[8];
    V[0] = (l & 1) ? 0xEEEE : 0x4444;
    V[1] = L_MEM + l16; V[2] = w * 4096 + l16; V[3] = V[2] + 65536;
    V[4] = L_QK + (4 * g + q) * 16 + (p & 1) * 8;
    V[5] = L_QK + (32 * w + i16) * 16 + g * 4;
    V[6] = (i16 & 8) ? 0xEEEE : 0x4444;
    V[7] = L_QK + 4096 + (32 * (g >> 1) + 8 * (g & 1) + 16 * (q >> 1) + 2 * (p >> 1) + (q & 1)) * 16 + (p & 1) * 8; V[8] = 0;
    const int r8 = i16 & 7;
    { const int tile = (g & 1) ? 1 : (i16 >> 3) * 2; const u32 o = (2 * (10 * c + t0 + tile) + (g >> 1)) * 4096 + h * 128 + r8 * 16; V[9] = WS_WINA + (((o >> 13) << 14) | selA | (o & 8191)); }
    { const u32 o = gb0 * 1024 + l16; V[10] = ((o >> 13) << 14) | selB | (o & 8191); V[11] = L_MEM + gb0 * 1024 + g * 256 + r8 * 32 + (i16 >> 3) * 16; }
    V[12] = L_VB + l16;
    u32 fl;
    if (NT == 3) {
        const u32 o = (l >> 5) * 2048 + (32 * c + (l & 31)) * 16;
        V[13] = L_QK + 2048 + o; V[14] = o + h * 4096;
        V[15] = h * 8192 + w * 4096 + l16; fl = w == 0;
    } else {
        const int u = (role >> 5) * 20 + (role & 31);
        V[13] = WS_WINA + selA + (u >> 1) * 16384 + (u & 1) * 4096 + l16;
        const u32 o = u * 128 + r8 * 16 + (g & 1) * 8; V[14] = ((o >> 13) << 14) | selB | (o & 8191);
        V[15] = 0; fl = w == 3 && (role & 31) < 20;
    }
#pragma unroll
    for (int i = 0; i < 10; ++i) { const u32 o = (w * 10 + i) * 1024 + l16; V[16 + i] = (o / 320) * (D_ * 2) + o % 320; }
    { u32 vo[4]; tile_offs<0>(vo, w, l, HE_ * 2); X[0] = vo[0]; X[1] = vo[1]; X[2] = vo[2]; X[3] = vo[3]; }
    const int r7 = ((q >> 1) << 2) | ((g & 1) << 1) | (q & 1);
    X[4] = (((g >> 1) << 4) | r7) * 256 + (((2 * w) ^ r7) * 32) + 8 * p; X[5] = X[4] ^ 32;
    X[6] = 65536 + (4 * g + q) * 320 + 8 * p + t0 * 32;
    X[7] = (l & 7) * 1280 + gb0 * 128 + (l >> 3) * 16;
    const u64 wsb = (u64)a.ws;
    v4u rs = {(u32)wsb, (u32)(wsb >> 32), 0x7FFFFFFFu, 0x00020000u};
    u64 pq = (u64)(a.Wq + h * 128), pk = (u64)(a.Wk + h * 128), pv = (u64)(a.Wv + h * 128), po = (u64)(a.Wo + (size_t)h * 128 * D_ + c * 160), pm = (u64)a.mem0;
    fl |= (a.dl & 0xffff00u) | ((NT == 3 ? a.dl >> 24 : a.dl & 0xff) << 24);
    fl = __builtin_amdgcn_readfirstlane(fl);
    u32 s22 = w * 4096, s23 = w * 10240, s24 = fb_ + C_KVP + h * 256, s25 = fb_ + C_CHP + h * 256 + c * 4;
    u32 s47 = WS_DBG + (role * 4 + w) * 256;
#define CH_OPS : "+{v0}"(V[0]), "+{v1}"(V[1]), "+{v2}"(V[2]), "+{v3}"(V[3]), "+{v4}"(V[4]), "+{v5}"(V[5]), "+{v6}"(V[6]), "+{v7}"(V[7]), "+{v8}"(V[8]), "+{v9}"(V[9]), \
        "+{v10}"(V[10]), "+{v11}"(V[11]), "+{v12}"(V[12]), "+{v13}"(V[13]), "+{v14}"(V[14]), "+{v15}"(V[15]), "+{v16}"(V[16]), "+{v17}"(V[17]), "+{v18}"(V[18]), "+{v19}"(V[19]), \
        "+{v20}"(V[20]), "+{v21}"(V[21]), "+{v22}"(V[22]), "+{v23}"(V[23]), "+{v24}"(V[24]), "+{v25}"(V[25]), \
        "+{v84}"(X[0]), "+{v85}"(X[1]), "+{v86}"(X[2]), "+{v87}"(X[3]), "+{v88}"(X[4]), "+{v89}"(X[5]), "+{v90}"(X[6]), "+{v91}"(X[7]) \
      : "{s[8:11]}"(rs), "{s[12:13]}"(pq), "{s[14:15]}"(pk), "{s[16:17]}"(pv), "{s[18:19]}"(po), "{s[20:21]}"(pm), "{s22}"(s22), "{s23}"(s23), "{s24}"(s24), "{s25}"(s25), "{s26}"(fl), "{s47}"(s47) \
      : CHAIN_CLOBBERS
    asm volatile("s_nop 4\ns_cmp_lt_u32 s22, 0x2000\ns_cbranch_scc0 CH2_%=\n" CHAIN_ASM_3 "CH2_%=:\n" CHAIN_ASM_2 CH_OPS);
    __builtin_unreachable();
}

#define TSTAMP(role, s, t)
#define TINIT0()
#define TACC(i)
#define TOUT(role, t)
#define TCNT(i)
#define KST(h, i)
#define KT() 0u
#define KOUT(h) (void)ks_
#define TINIT() __builtin_amdgcn_s_waitcnt(0); TINIT0()
DEV v4u pack8(f4 a, f4 b) { const v4u o = {pk2(a[0], a[1]), pk2(a[2], a[3]), pk2(b[0], b[1]), pk2(b[2], b[3])}; return o; }
#define KVX 0
DEV void wait_ln(const rsrc_t rs, const u32 fb_, const int l) {
    for (u32 spins = 0;; ++spins) {
        if (__builtin_amdgcn_ballot_w64(gld32(rs, l * 4, fb_ + C_LNP) == 0) == 0) break;
        if (spins > SPIN_CAP) { gst32(rs, 2, 0, WS_CTL + C_ERR); break; }
        __builtin_amdgcn_s_sleep(4);
    }
    MEMFENCE();
}
DEV void kv_pull(const Args& a, const int h, const int w, const int l) {
    const unsigned char* src = (const unsigned char*)((w & 1) ? a.Wv : a.Wk) + h * 256;
    const u32 vo = (256 + 32 * (w >> 1) + (l >> 1)) * (HE_ * 2) + (l & 1) * 128;
    u32 d[2];
#pragma unroll
    for (int i = 0; i < 6; ++i) asm volatile("global_load_dword %0, %1, %2" : "=v"(d[i & 1]) : "v"(vo), "s"(src + (size_t)i * 64 * HE_ * 2) : "memory");
    asm volatile("s_waitcnt vmcnt(0)" : "+v"(d[0]), "+v"(d[1]) :: "memory");
}
DEV u32 ln_part(const Args& a, const rsrc_t rs, const int sg, const int w, const int l) {
    const int r = l & 15, g = l >> 4;
    LAS float* red = (LAS float*)(uintptr_t)162816;
    v4u v[2][5];
#pragma unroll
    for (int i = 0; i < 10; ++i) v[i / 5][i % 5] = *(const v4u*)(a.x + (size_t)(sg * 32 + (i / 5) * 16 + r) * D_ + w * 160 + g * 8 + (i % 5) * 32);
    const u32 fb_ = flag_base(a);
    float f[2][40], sm[2] = {0, 0}, qs[2] = {0, 0};
#pragma unroll
    for (int b = 0; b < 2; ++b) {
#pragma unroll
        for (int i = 0; i < 20; ++i) { f[b][2 * i] = asf(v[b][i >> 2][i & 3] << 16); f[b][2 * i + 1] = asf(v[b][i >> 2][i & 3] & 0xffff0000u); }
#pragma unroll
        for (int i = 0; i < 40; ++i) sm[b] += f[b][i];
        sm[b] = allsum_g(sm[b]);
        if (g == 0) red[b * 64 + w * 16 + r] = sm[b];
    }
    BAR();
#pragma unroll
    for (int b = 0; b < 2; ++b) {
        const float mean = (red[b * 64 + r] + red[b * 64 + 16 + r] + red[b * 64 + 32 + r] + red[b * 64 + 48 + r]) * (1.0f / D_);
#pragma unroll
        for (int i = 0; i < 40; ++i) { f[b][i] -= mean; qs[b] += f[b][i] * f[b][i]; }
        qs[b] = allsum_g(qs[b]);
        if (g == 0) red[128 + b * 64 + w * 16 + r] = qs[b];
    }
    BAR();
#pragma unroll
    for (int b = 0; b < 2; ++b) {
        const float rstd = 1.0f / sqrtf((red[128 + b * 64 + r] + red[128 + b * 64 + 16 + r] + red[128 + b * 64 + 32 + r] + red[128 + b * 64 + 48 + r]) * (1.0f / D_) + 1e-5f);
#pragma unroll
        for (int i = 0; i < 5; ++i) {
            v4u o;
#pragma unroll
            for (int j = 0; j < 4; ++j) o[j] = pk2(f[b][i * 8 + 2 * j] * rstd, f[b][i * 8 + 2 * j + 1] * rstd);
            gst128(rs, o, WS_XN + sg * 40960 + b * 20480 + w * 5120 + l * 16 + i * 1024, 0);
        }
    }
    WAIT_V(0); BAR();
    if (w == 0 && l == 0) gst32(rs, 1, 0, fb_ + C_LNP + sg * 4);
    return fb_;
}
DEV void kv_role(const Args& a, const rsrc_t rs, const int h, const int w, const int l) {
    const int g = l >> 4, q = (l >> 2) & 3, p = l & 3;
    b8 W[4][20]; u32 fb_;
    u32 ks_[12]; ks_[7] = KT();
    const u32 xn = WS_XN + w * 10240 + l * 16;
    auto xissue = [&](int s, int slot) {
#pragma unroll
        for (int i = 0; i < 10; ++i) wdmas(rs, xn, s * 40960 + i * 1024, slot * 40960 + w * 10240 + i * 1024);
    };
    {
        u32 vo[4]; tile_offs<1>(vo, w, l, HE_ * 2);
        const unsigned char* wk = (const unsigned char*)(a.Wk + h * 128); const unsigned char* wv = (const unsigned char*)(a.Wv + h * 128);
        auto src = [&](int G, int j) { return ((j & 2) ? wv : wk) + (size_t)(2 * G + (j & 1)) * 64 * HE_ * 2; };
        v4u R[48];
        auto rawld = [&](int G) {
#pragma unroll
            for (int j = 0; j < 16; ++j) R[(G - 2) * 16 + j] = alda(src(G, j >> 2), vo[j & 3]);
        };
        const int r7 = ((g & 1) << 2) | q;
        const u32 fb = (((g >> 1) << 4) | r7) * 256 + 8 * p + (w >> 1) * 2 * 16384;
        ks_[8] = ks_[9] = ks_[10] = ks_[11] = 0;
#pragma unroll
        for (int j = 0; j < 8; ++j) stage_tile(src(j >> 2, j & 3), vo, j * 16384, w);
        fb_ = flag_base(a);
        ks_[0] = KT();
#pragma unroll
        for (int G = 0; G < 5; ++G) {
            if (G < 2) WAIT_V(16);
            else {
                if (G == 2) { WAIT_A8(16, R, 0); WAIT_A8(16, R, 8); } else if (G == 3) { WAIT_A8(16, R, 16); WAIT_A8(16, R, 24); } else { WAIT_A8(0, R, 32); WAIT_A8(0, R, 40); }
#pragma unroll
                for (int j = 0; j < 16; ++j) lds_w128(((G & 1) * 4 + (j >> 2)) * 16384 + (w * 4 + (j & 3)) * 1024 + l * 16, R[(G - 2) * 16 + j]);
                if (G == 4 && w == 0) wait_ln(rs, fb_, l);
            }
            BAR(); ks_[1 + G] = KT();
            if (G == 4) KOUT(h);
            if (G == 4) xissue(0, 2);
#pragma unroll
            for (int j = 0; j < 2; ++j)
#pragma unroll
                for (int ks = 0; ks < 2; ++ks)
#pragma unroll
                    for (int t = 0; t < 4; ++t) { const u32 o = fb + ((G & 1) * 4 + j) * 16384 + ks * 8192 + (((4 * (w & 1) + t) ^ r7) * 32);
                        W[t][4 * G + 2 * j + ks] = cat(lds_tr(o), lds_tr(o + 2048)); }
            if (G < 3) rawld(G + 2);
        }
    }
    BAR();
    xissue(1, 0);
    WAIT_V(10); BAR(); KST(h, 6);
    const u32 dst = ((w & 2) ? WS_VF : WS_KF) + h * 8192 + (w & 1) * 2048 + l * 16;
    int slot = 2;
    TINIT0();
    b8 xf[5];
#pragma unroll
    for (int i = 0; i < 5; ++i) xf[i] = asb8(lds_r128(81920 + l * 16 + (i % 3) * 1024));
    for (int s = 0; s < NSEG; ++s) {
        const int nslot = slot == 2 ? 0 : slot + 1, dslot = slot == 0 ? 2 : slot - 1, sd = s + 2 < NSEG ? s + 2 : s;
        const u32 xb = slot * 40960 + l * 16, xnb = nslot * 40960 + l * 16;
        f4 acc[2][4];
        auto outp = [&](int tt, int j) { if (KVX == 2 && s != 63) { asm volatile("" :: "v"(acc[tt][2 * j]), "v"(acc[tt][2 * j + 1])); return; } gst128(rs, pack8(acc[tt][2 * j], acc[tt][2 * j + 1]), dst + tt * 4096 + j * 1024, s * 262144); };
#pragma unroll
        for (int i = 0; i < 40; ++i) {
            xf[(i + 3) % 5] = asb8(lds_r128(i + 3 < 40 ? xb + (i + 3) * 1024 : xnb + (i - 37) * 1024));
            asm volatile("" :: "v"(xf[(i + 4) % 5]));
            if (i == 18) { WAIT_V(0); __builtin_amdgcn_s_barrier(); MEMFENCE(); if (s > 0 && w == 0 && l == 0) gst32(rs, s, 0, fb_ + C_KVP + h * 256); TSTAMP(128 + h, s, w * 64 + l); }
            if (KVX != 1 && i >= 19 && (i & 1) && i < 39) wdmas(rs, xn, sd * 40960 + ((i - 19) >> 1) * 1024, dslot * 40960 + w * 10240 + ((i - 19) >> 1) * 1024);
            if (i == 22 || i == 24) outp(0, (i - 22) >> 1);
            SB();
#pragma unroll
            for (int t = 0; t < 4; ++t) acc[i / 20][t] = mfma(W[t][i % 20], xf[i % 5], i % 20 ? acc[i / 20][t] : f4{0, 0, 0, 0});
            SB();
        }
        outp(1, 0); outp(1, 1);
        slot = nslot;
        TACC(4);
    }
    TOUT(128 + h, w * 64 + l);
    WAIT_V(0); BAR();
    if (w == 0 && l == 0) gst32(rs, NSEG, 0, fb_ + C_KVP + h * 256);
}

#define QSTART -1
#define QD 1
#define WSTART 0
#define FSTART 2
DEV v2u gld64(rsrc_t r, u32 vo, u32 so) { return __builtin_amdgcn_raw_buffer_load_b64(r, vo, so, SC1); }
DEV v2u lds_r64(u32 o) { return *(LAS v2u*)(uintptr_t)o; }
DEV f4 mfma16(v2u a, v2u b, f4 c) { typedef short s4 __attribute__((ext_vector_type(4))); return __builtin_amdgcn_mfma_f32_16x16x16bf16_1k(__builtin_bit_cast(s4, a), __builtin_bit_cast(s4, b), c, 0, 0, 0); }
template <int R> DEV float ror(float v) { return asf(__builtin_amdgcn_update_dpp(0, asu(v), 0x120 + R, 0xf, 0xf, true)); }
DEV float wave_sum(float v) { v += ror<1>(v); v += ror<2>(v); v += ror<4>(v); v += ror<8>(v); return allsum_g(v); }
template <int R> DEV u32 roru(u32 v) { return __builtin_amdgcn_update_dpp(0, v, 0x120 + R, 0xf, 0xf, true); }
#define PSLEEP 2
template <int SL = PSLEEP> DEV void wait_flags(const rsrc_t rs, const u32 off, const int n, const int l, const u32 s, u32& known) {
    if (known > s) return;
    const u32 vo = (n == 14 ? ((l & 15) < 14 ? l & 15 : 0) : l & (n - 1)) * 4;
    u32 v;
    for (u32 spins = 0;; ++spins) {
        v = gld32(rs, vo, off);
        if (__builtin_amdgcn_ballot_w64(v <= s) == 0) break;
        if (spins > SPIN_CAP) { gst32(rs, 2, 0, WS_CTL + C_ERR); break; }
        __builtin_amdgcn_s_sleep(SL);
    }
    if (n > 1) v = min(v, roru<1>(v));
    if (n > 2) v = min(v, roru<2>(v));
    if (n > 4) v = min(v, roru<4>(v));
    if (n > 8) v = min(v, roru<8>(v));
    known = __builtin_amdgcn_readlane(v, 0);
    if (n > 16) known = min(known, (u32)__builtin_amdgcn_readlane(v, 31));
    MEMFENCE();
}
DEV void publish(const rsrc_t rs, const u32 off, const u32 v, const int t) { WAIT_V(0); BAR(); if (t == 0) gst32(rs, v, 0, off); }

DEV void load_panel(b8 (&W)[2][20], const unsigned char* src, const int ld, const u32 cmask, const int w, const int l) {
    const int g = l >> 4, q = (l >> 2) & 3, p = l & 3;
    u32 vo[4];
#pragma unroll
    for (int i = 0; i < 4; ++i) { const int rho = 16 * w + 4 * i + (l >> 4), ch = l & 15; vo[i] = rho2row<1>(rho) * ld + (((((ch >> 1) ^ (rho & 7)) * 32) + (ch & 1) * 16) & cmask); }
    const int r7 = ((g & 1) << 2) | q;
    const u32 fb = (((g >> 1) << 4) | r7) * 256 + 8 * p;
#pragma unroll
    for (int j = 0; j < 8; ++j) stage_tile(src + (size_t)j * 64 * ld, vo, j * 16384, w);
    v4u R[8];
#pragma unroll
    for (int j = 0; j < 8; ++j) R[j] = alda(src + (size_t)(8 + (j >> 2)) * 64 * ld, vo[j & 3]);
#pragma unroll
    for (int G = 0; G < 3; ++G) {
        if (G == 0) WAIT_V(24); else if (G == 1) WAIT_V(8);
        else {
            WAIT_A8(0, R, 0);
#pragma unroll
            for (int j = 0; j < 8; ++j) lds_w128((j >> 2) * 16384 + (w * 4 + (j & 3)) * 1024 + l * 16, R[j]);
        }
        BAR();
#pragma unroll
        for (int j = 0; j < (G < 2 ? 4 : 2); ++j)
#pragma unroll
            for (int ks = 0; ks < 2; ++ks)
#pragma unroll
                for (int t = 0; t < 2; ++t) { const u32 o = fb + ((G & 1) * 4 + j) * 16384 + ks * 8192 + (((2 * w + t) ^ r7) * 32);
                    W[t][8 * G + 2 * j + ks] = cat(lds_tr(o), lds_tr(o + 2048)); }
    }
    BAR();
}
#define KEEP(x) asm volatile("" :: "v"(x))
template <int IMG = 0, class F> DEV void panel_mm(f4 (&acc)[2][2], const b8 (&W)[2][20], const u32 xb, F fill) {
    b8 xf[9];
#pragma unroll
    for (int i = 0; i < 6; ++i) xf[i] = asb8(lds_r128(IMG ? ((i & 2) ? xb ^ 64 : xb) + (i & 1) * 20480 + (i >> 2) * 1024 : xb + (i & 1) * 20480 + (i >> 1) * 1024));
#pragma unroll
    for (int i = 0; i < 40; ++i) {
        if (i + 6 < 40) xf[(i + 6) % 9] = asb8(lds_r128(IMG ? (((i + 6) & 2) ? xb ^ 64 : xb) + (i & 1) * 20480 + ((i + 6) >> 2) * 1024 : xb + (i & 1) * 20480 + ((i + 6) >> 1) * 1024));
        if (i >= 1 && i + 6 < 40) KEEP(xf[(i + 8) % 9]);
        if (i >= 2 && i + 6 < 40) KEEP(xf[(i + 7) % 9]);
        fill(i);
        SB();
#pragma unroll
        for (int t = 0; t < 2; ++t) acc[i & 1][t] = mfma(W[t][i >> 1], xf[i % 9], acc[i & 1][t]);
        SB();
    }
}

DEV u32 ald32(rsrc_t r, u32 vo, u32 so) { u32 v; asm volatile("buffer_load_dword %0, %1, %2, %3 offen sc1" : "=v"(v) : "v"(vo), "s"(r), "s"(so) : "memory"); return v; }
DEV v2u ald64(rsrc_t r, u32 vo, u32 so) { v2u v; asm volatile("buffer_load_dwordx2 %0, %1, %2, %3 offen sc1" : "=v"(v) : "v"(vo), "s"(r), "s"(so) : "memory"); return v; }
#define WAIT_VR(n, x) asm volatile("s_waitcnt vmcnt(" #n ")" : "+v"(x) :: "memory")
DEV void qa_role(const Args& a, const rsrc_t rs, const int h, const int w, const int l) {
    constexpr u32 L_Q = 81920, L_KV = 90112, L_M = 122880, L_F = 126976;
    const int i16 = l & 15, g = l >> 4, q = (l >> 2) & 3, p = l & 3, qt = w & 1, eh = w >> 1, t = w * 64 + l;
    u32 kvk = 0;
    if (QD) kv_pull(a, h, w, l);
    if (QSTART >= 0) wait_flags<32>(rs, flag_base(a) + C_KVP + h * 256, 1, l, QSTART, kvk);
    b8 W[2][20];
    load_panel(W, (const unsigned char*)(a.Wq + h * 128), HE_ * 2, ~0u, w, l);
    const u32 fb_ = flag_base(a); KST(32 + h, 0);
    wait_flags<8>(rs, fb_ + C_KVP + h * 256, 1, l, 0, kvk);
    const u32 xn = WS_XN + w * 10240 + l * 16;
    const u32 kv = ((w & 2) ? WS_VF : WS_KF) + h * 8192 + (w & 1) * 4096 + l * 16, km = WS_KVM + h * 4096 + w * 1024 + l * 16;
#pragma unroll
    for (int i = 0; i < 10; ++i) wdmas(rs, xn, i * 1024, w * 10240 + i * 1024);
#pragma unroll
    for (int i = 0; i < 4; ++i) wdmas(rs, kv, i * 1024, L_KV + w * 4096 + i * 1024);
    WAIT_V(0); BAR();
    TINIT();
    const float cs = 0.08838834764831845f * 1.4426950408889634f;
    constexpr int QPUB = 22, QFL = 12;
    for (int s = 0; s < NSEG; ++s) {
        const int sn = s + 1 < NSEG ? s + 1 : s - 1;
        const u32 kb = L_KV + (s & 1) * 16384;
        u32 fv;
        TACC(0);
        {
            f4 acc[2][2] = {};
            panel_mm(acc, W, (s & 1) * 40960 + l * 16, [&](int i) {
                if (i == QFL) fv = ald32(rs, w == 0 ? (l & 3) * 4 : 0x80000000u, fb_ + C_CHP + h * 256);
                if (i == QPUB) { WAIT_V(6); __builtin_amdgcn_s_barrier(); MEMFENCE(); if (s > 0 && t == 0) gst32(rs, s, 0, fb_ + C_QAP + (h >> 1) * 256 + (h & 1) * 4); }
                if ((i & 3) == 2) wdmas(rs, xn, sn * 40960 + (i >> 2) * 1024, (sn & 1) * 40960 + w * 10240 + (i >> 2) * 1024);
            });
#pragma unroll
            for (int tt = 0; tt < 2; ++tt) lds_w128(L_Q + tt * 4096 + w * 1024 + l * 16, pack8(acc[tt][0], acc[tt][1]));
        }
        TACC(1); WAIT_VR(7, fv);
        fv = min(fv, roru<1>(fv)); fv = min(fv, roru<2>(fv));
        if (t == 0) *(LAS u32*)(uintptr_t)L_F = fv;
        BAR(); TACC(3);
        const bool early = __builtin_amdgcn_readfirstlane(*(LAS u32*)(uintptr_t)L_F) > (u32)s;
        if (early) wdmas(rs, km, s * 131072, L_M + w * 1024);
        wait_flags(rs, fb_ + C_KVP + h * 256, 1, l, sn, kvk);
        b8 qf[4]; f4 S1 = {}, S2 = {}, S0 = {};
#pragma unroll
        for (int ks = 0; ks < 4; ++ks) {
            qf[ks] = asb8(lds_r128(L_Q + qt * 4096 + ks * 1024 + l * 16));
            const b8 k1 = asb8(lds_r128(kb + ks * 1024 + l * 16)), k2 = asb8(lds_r128(kb + 4096 + ks * 1024 + l * 16));
            wdmas(rs, kv, sn * 262144 + ks * 1024, L_KV + (sn & 1) * 16384 + w * 4096 + ks * 1024);
            S1 = mfma(k1, qf[ks], S1);
            S2 = mfma(k2, qf[ks], S2);
        }
        b8 vt[4];
#pragma unroll
        for (int e = 0; e < 4; ++e) { const u32 o = kb + 8192 + (2 * eh + (e >> 1)) * 1024 + (p * 16 + 4 * g + q) * 16 + (e & 1) * 8; vt[e] = cat(lds_tr(o), lds_tr(o + 4096)); }
        float m = -3.0e38f;
#pragma unroll
        for (int i = 0; i < 4; ++i) {
            if (4 * g + i > 16 * qt + i16) S1[i] = -3.0e38f;
            if (16 + 4 * g + i > 16 * qt + i16) S2[i] = -3.0e38f;
            m = fmaxf(m, fmaxf(S1[i], S2[i]));
        }
        TACC(4);
        if (early) WAIT_V(4);
        else if (w == 0) {
            u32 c2 = 0;
            wait_flags(rs, fb_ + C_CHP + h * 256, 4, l, s, c2);
            TACC(5);
#pragma unroll
            for (int i = 0; i < 4; ++i) wdmas(rs, km, s * 131072 + i * 1024, L_M + i * 1024);
            WAIT_V(0);
        }
        BAR(); TACC(7);
#pragma unroll
        for (int ks = 0; ks < 4; ++ks) { const u32 o = L_M + (4 * g + q) * 16 + (p & 1) * 8 + ks * 512; S0 = mfma(cat(lds_tr(o), lds_tr(o + 256)), qf[ks], S0); }
        v2u vm[4];
#pragma unroll
        for (int e = 0; e < 4; ++e) vm[e] = lds_r64(L_M + 2048 + (64 * eh + 16 * e + i16) * 16 + (g & 1) * 8);
        m = fmaxf(fmaxf(m, fmaxf(S0[0], S0[1])), fmaxf(S0[2], S0[3]));
        m = allmax_g(m) * cs;
#pragma unroll
        for (int i = 0; i < 4; ++i) {
            S1[i] = __builtin_amdgcn_exp2f(fmaf(S1[i], cs, -m)); S2[i] = __builtin_amdgcn_exp2f(fmaf(S2[i], cs, -m));
            S0[i] = g < 2 ? __builtin_amdgcn_exp2f(fmaf(S0[i], cs, -m)) : 0.f;
        }
        const v4u p12 = pack8(S1, S2); const v2u p0 = {pk2(S0[0], S0[1]), pk2(S0[2], S0[3])};
        float sum = 0;
#pragma unroll
        for (int i = 0; i < 4; ++i) sum += asf(p12[i] << 16) + asf(p12[i] & 0xffff0000u);
        sum += asf(p0[0] << 16) + asf(p0[0] & 0xffff0000u) + asf(p0[1] << 16) + asf(p0[1] & 0xffff0000u);
        const float inv = __builtin_amdgcn_rcpf(allsum_g(sum));
        f4 O[4];
#pragma unroll
        for (int e = 0; e < 4; ++e) { O[e] = mfma(vt[e], asb8(p12), f4{0, 0, 0, 0}); O[e] = mfma16(vm[e], p0, O[e]); O[e] *= inv; }
#pragma unroll
        for (int j = 0; j < 2; ++j) gst128(rs, pack8(O[2 * j], O[2 * j + 1]), WS_OF + h * 8192 + qt * 4096 + (2 * eh + j) * 1024 + l * 16, s * 262144);
        if (early) TCNT(2);
        TSTAMP(160 + h, s, t); TACC(6);
    }
    publish(rs, fb_ + C_QAP + (h >> 1) * 256 + (h & 1) * 4, NSEG, t);
    TOUT(160 + h, t);
}

DEV void wo_role(const Args& a, const rsrc_t rs, const u32 fb_, const int idx, const int w, const int l) {
    const int kg = idx & 3, cg = idx >> 2, g = l >> 4, q = (l >> 2) & 3, p = l & 3, t = w * 64 + l, h0 = 8 * kg + 2 * w;
    u32 kn = 0;
    wait_flags<32>(rs, fb_ + C_KVP + idx * 256, 1, l, WSTART, kn); kn = 0;
    b8 W[8][5];
#pragma unroll
    for (int hh = 0; hh < 2; ++hh) {
        const unsigned char* src = (const unsigned char*)(a.Wo + (size_t)(h0 + hh) * 128 * D_ + cg * 80);
#pragma unroll
        for (int i = 0; i < 20; ++i) { const u32 o = i * 1024 + l * 16; glds16(src + (o / 160) * (D_ * 2) + o % 160, w * 40960 + hh * 20480 + i * 1024); }
    }
#pragma unroll
    for (int hh = 0; hh < 2; ++hh) {
        if (hh == 0) WAIT_V(20); else WAIT_V(0);
#pragma unroll
        for (int ks = 0; ks < 4; ++ks)
#pragma unroll
            for (int ct = 0; ct < 5; ++ct) { const u32 o = w * 40960 + hh * 20480 + (32 * ks + 4 * g + q) * 160 + ct * 32 + 8 * p; W[hh * 4 + ks][ct] = cat(lds_tr(o), lds_tr(o + 16 * 160)); }
    }
    BAR();
    u32 ro[2], go[2];
#pragma unroll
    for (int r = 0; r < 2; ++r) { const int id = min(t + 256 * r, 319), tok = id / 10, j = id % 10;
        ro[r] = tok * 336 + j * 32; go[r] = t + 256 * r < 320 ? WS_WP + kg * 40960 + tok * 1280 + cg * 160 + j * 16 : 0x80000000u; }
#pragma unroll
    for (int i = 0; i < 40; ++i) asm volatile("" : "+a"(W[i / 5][i % 5]));
    TINIT();
    constexpr int WOD = 14;
    constexpr u32 L_F = 43008;
    bool pre = false;
    for (int s = 0; s < NSEG; ++s) {
        v4u B[16];
        auto ld = [&](int i) { B[i] = gld128(rs, WS_OF + h0 * 8192 + l * 16 + (i & 3) * 1024, s * 262144 + (i >> 2) * 4096); };
        TACC(0); if (!pre) wait_flags(rs, fb_ + C_QAP + h0 * 128, 2, l, s, kn); TACC(1);
#pragma unroll
        for (int i = 0; i < WOD; ++i) { ld(i); SB(); }
        f4 acc[2][5] = {};
        u32 fv;
#pragma unroll
        for (int i = 0; i < 16; ++i) {
#pragma unroll
            for (int ct = 0; ct < 5; ++ct) acc[(i >> 2) & 1][ct] = mfma(W[(i >> 3) * 4 + (i & 3)][ct], asb8(B[i]), acc[(i >> 2) & 1][ct]);
            if (i + WOD < 16) ld(i + WOD);
            if (i == 2 && pre) { __builtin_amdgcn_s_barrier(); MEMFENCE(); if (t == 0) gst32(rs, s, 0, fb_ + C_WOP + idx * 4); }
            if (i == 8) fv = ald32(rs, (l & 1) * 4, fb_ + C_QAP + h0 * 128);
            SB();
        }
        const u32 lb = 0;
#pragma unroll
        for (int i = 0; i < 10; ++i) lds_w128(lb + w * 10752 + ((i / 5) * 16 + (l & 15)) * 336 + (i % 5) * 64 + g * 16, __builtin_bit_cast(v4u, acc[i / 5][i % 5]));
        WAIT_VR(0, fv);
        fv = min(fv, roru<1>(fv));
        if (l == 0) *(LAS u32*)(uintptr_t)(L_F + w * 4) = fv;
        TACC(3); BAR(); TACC(4);
        f4 x[2][4];
#pragma unroll
        for (int r = 0; r < 2; ++r)
#pragma unroll
            for (int j = 0; j < 4; ++j) x[r][j] = __builtin_bit_cast(f4, lds_r128(lb + ro[0] + r * 16 + j * 10752));
        const v4u fa = lds_r128(L_F);
        SB();
        gst128(rs, pack8((x[0][0] + x[0][1]) + (x[0][2] + x[0][3]), (x[1][0] + x[1][1]) + (x[1][2] + x[1][3])), go[0], s * 163840);
        if (w == 0) {
#pragma unroll
            for (int r = 0; r < 2; ++r)
#pragma unroll
                for (int j = 0; j < 4; ++j) x[r][j] = __builtin_bit_cast(f4, lds_r128(lb + ro[1] + r * 16 + j * 10752));
            SB();
            gst128(rs, pack8((x[0][0] + x[0][1]) + (x[0][2] + x[0][3]), (x[1][0] + x[1][1]) + (x[1][2] + x[1][3])), go[1], s * 163840);
        }
        pre = __builtin_amdgcn_readfirstlane(min(min(fa[0], fa[1]), min(fa[2], fa[3]))) > (u32)s + 1;
        if (pre) TCNT(7);
        TSTAMP(192 + idx, s, t); TACC(5);
        if (!pre) publish(rs, fb_ + C_WOP + idx * 4, s + 1, t);
    }
    TOUT(192 + idx, t);
}

DEV void gst64(rsrc_t r, v2u v, u32 vo, u32 so) { __builtin_amdgcn_raw_buffer_store_b64(v, r, vo, so, STAUX); }
DEV void red_role(const Args& a, const rsrc_t rs, const u32 fb_, const int idx, const int w, const int l) {
    const int tok = idx * 4 + w;
    const rsrc_t rx = __builtin_amdgcn_make_buffer_rsrc((void*)a.x, 0, S_ * D_ * 2, 0x00020000);
    const u32 oob = l < 32 ? 0u : 0x80000000u;
    u32 kn = 0;
    TINIT();
    for (int s = 0; s < NSEG; ++s) {
        v2u xv[3];
#pragma unroll
        for (int i = 0; i < 3; ++i) xv[i] = __builtin_amdgcn_raw_buffer_load_b64(rx, (tok * 1280 + l * 8 + i * 512) | (i == 2 ? oob : 0u), s * 40960, 0);
        TACC(0); if (w == 0) wait_flags(rs, fb_ + C_WOP, 32, l, s, kn); BAR(); TACC(1);
        v2u pv[4][3];
#pragma unroll
        for (int i = 0; i < 3; ++i)
#pragma unroll
            for (int k = 0; k < 4; ++k) pv[k][i] = gld64(rs, (WS_WP + tok * 1280 + l * 8 + i * 512) | (i == 2 ? oob : 0u), s * 163840 + k * 40960);
        SB();
        float f[12]; v2u hb[3]; float sm = 0;
#pragma unroll
        for (int i = 0; i < 3; ++i) {
            auto up = [](v2u v) { return f4{asf(v[0] << 16), asf(v[0] & 0xffff0000u), asf(v[1] << 16), asf(v[1] & 0xffff0000u)}; };
            const f4 o = (up(pv[0][i]) + up(pv[1][i])) + (up(pv[2][i]) + up(pv[3][i]));
#pragma unroll
            for (int j = 0; j < 2; ++j) {
                const u32 ob = pk2(o[2 * j], o[2 * j + 1]);
                hb[i][j] = pk2(asf(xv[i][j] << 16) + asf(ob << 16), asf(xv[i][j] & 0xffff0000u) + asf(ob & 0xffff0000u));
                f[4 * i + 2 * j] = asf(hb[i][j] << 16); f[4 * i + 2 * j + 1] = asf(hb[i][j] & 0xffff0000u); sm += f[4 * i + 2 * j] + f[4 * i + 2 * j + 1];
            }
        }
        const float mean = wave_sum(sm) * (1.0f / D_);
        float qs = 0;
#pragma unroll
        for (int i = 0; i < 12; ++i) { f[i] = (i < 8 || l < 32) ? f[i] - mean : 0.f; qs += f[i] * f[i]; }
        const float rstd = 1.0f / sqrtf(wave_sum(qs) * (1.0f / D_) + 1e-5f);
        TACC(7);
#pragma unroll
        for (int i = 0; i < 3; ++i) gst64(rs, v2u{pk2(f[4 * i] * rstd, f[4 * i + 1] * rstd), pk2(f[4 * i + 2] * rstd, f[4 * i + 3] * rstd)}, (WS_HN + tok * 1280 + l * 8 + i * 512) | (i == 2 ? oob : 0u), s * 40960);
#pragma unroll
        for (int i = 0; i < 3; ++i) gst64(rs, hb[i], (WS_HB + tok * 1280 + l * 8 + i * 512) | (i == 2 ? oob : 0u), s * 40960);
        TACC(2); publish(rs, fb_ + C_RDP + idx * 4, s + 1, w * 64 + l); TSTAMP(224 + idx, s, w * 64 + l); TACC(3);
    }
    TOUT(224 + idx, w * 64 + l);
}

DEV void f1_role(const Args& a, const rsrc_t rs, const u32 fb_, const int idx, const int w, const int l) {
    u32 kn = 0;
    wait_flags<32>(rs, fb_ + C_KVP + idx * 256, 1, l, FSTART, kn); kn = 0;
    b8 W[2][20];
    load_panel(W, (const unsigned char*)(a.w1 + idx * 128), FF_ * 2, idx == 13 ? 127u : ~0u, w, l);
    const u32 hn = WS_HN + (w * 8 + (l >> 3)) * 1280 + (l & 7) * 16;
    const u32 hw = w * 10240 + (((l >> 3) ^ (w & 1)) * 128) + (((l & 7) ^ (l >> 3)) * 16);
    const u32 xo = ((l >> 3) & 1) * 10240 + (((l & 7) ^ ((l >> 3) & 1)) * 128) + (((l >> 4) ^ (l & 7)) * 16);
    const u32 ud = idx * 4 + w < 54 ? WS_UF + (idx * 4 + w) * 2048 + l * 16 : 0x80000000u;
    const int t = w * 64 + l;
    constexpr u32 L_F = 81920;
    bool pre = false;
    TINIT();
    for (int s = 0; s < NSEG; ++s) {
        if (!pre) {
            if (s > 0) publish(rs, fb_ + C_F1P + idx * 4, s, t);
            TACC(0); if (w == 0) wait_flags(rs, fb_ + C_RDP, 8, l, s, kn); BAR(); TACC(1);
        }
        v4u H[10];
#pragma unroll
        for (int i = 0; i < 10; ++i) { H[i] = gld128(rs, hn, s * 40960 + i * 128); SB(); }
#pragma unroll
        for (int i = 0; i < 10; ++i) lds_w128(hw + i * 1024, H[i]);
        BAR();
        if (pre && t == 0) gst32(rs, s, 0, fb_ + C_F1P + idx * 4);
        TACC(2);
        f4 acc[2][2] = {};
        u32 fv;
        panel_mm<1>(acc, W, xo, [&](int i) { if (i == 16) fv = ald32(rs, w == 0 ? (l & 7) * 4 : 0x80000000u, fb_ + C_RDP); });
        TACC(3);
#pragma unroll
        for (int tt = 0; tt < 2; ++tt) {
#pragma unroll
            for (int i = 0; i < 4; ++i) { acc[tt][0][i] = fmaxf(acc[tt][0][i], 0.f); acc[tt][1][i] = fmaxf(acc[tt][1][i], 0.f); }
            gst128(rs, pack8(acc[tt][0], acc[tt][1]), ud + tt * 1024, s * 110592);
        }
        WAIT_VR(2, fv);
        fv = min(fv, roru<1>(fv)); fv = min(fv, roru<2>(fv)); fv = min(fv, roru<4>(fv));
        if (t == 0) *(LAS u32*)(uintptr_t)L_F = fv;
        BAR();
        pre = __builtin_amdgcn_readfirstlane(*(LAS u32*)(uintptr_t)L_F) > (u32)s + 1;
        if (pre) TCNT(7);
        TSTAMP(232 + idx, s, t); TACC(4);
    }
    publish(rs, fb_ + C_F1P + idx * 4, NSEG, t);
    TOUT(232 + idx, t);
}

DEV void f2_role(const Args& a, const rsrc_t rs, const u32 fb_, const int idx, const int w, const int l) {
    const int g = l >> 4, q = (l >> 2) & 3, p = l & 3, t = w * 64 + l, k0 = w == 3 ? 40 : 14 * w;
    u32 kn = 0;
    wait_flags<32>(rs, fb_ + C_KVP + (idx + 14) * 256, 1, l, FSTART, kn); kn = 0;
    b8 W[14][4];
    const unsigned char* src = (const unsigned char*)(a.w2 + (size_t)(k0 * 32 + (l >> 3)) * D_ + idx * 64) + (l & 7) * 16;
#pragma unroll
    for (int hf = 0; hf < 2; ++hf) {
#pragma unroll
        for (int i = 0; i < 28; ++i) glds16(src + (size_t)(hf * 224 + i * 8) * (D_ * 2), w * 28672 + i * 1024);
        WAIT_V(0);
#pragma unroll
        for (int ks = 0; ks < 7; ++ks)
#pragma unroll
            for (int ct = 0; ct < 4; ++ct) { const u32 o = w * 28672 + (32 * ks + 4 * g + q) * 128 + ct * 32 + 8 * p; W[hf * 7 + ks][ct] = cat(lds_tr(o), lds_tr(o + 2048)); }
        WAIT_L0();
    }
    if (w == 3) {
#pragma unroll
        for (int ct = 0; ct < 4; ++ct) { W[0][ct] = asb8(v4u{0, 0, 0, 0}); W[1][ct] = asb8(v4u{0, 0, 0, 0}); }
    }
    BAR();
    u32 ro[2], go[2];
#pragma unroll
    for (int r = 0; r < 2; ++r) { const int id = t + 256 * r, tok = id >> 4, j = id & 15;
        ro[r] = tok * 272 + j * 16; go[r] = tok * 1280 + idx * 128 + j * 8; }
#pragma unroll
    for (int i = 0; i < 56; ++i) asm volatile("" : "+a"(W[i >> 2][i & 3]));
    TINIT();
    constexpr int F2D = 16;
    bool pre = false;
    for (int s = 0; s < NSEG; ++s) {
        v4u B[28];
        auto ld = [&](int i) { B[i] = gld128(rs, WS_UF + k0 * 2048 + l * 16 + (i & 3) * 1024, s * 110592 + (i >> 2) * 4096); };
        if (!pre) { if (w == 0) wait_flags(rs, fb_ + C_F1P, 14, l, s, kn); BAR(); }
        TACC(1);
        u32 fv;
#pragma unroll
        for (int i = 0; i < F2D; ++i) { ld(i); SB(); }
        f4 acc[2][4] = {};
        v2u hb[2];
#pragma unroll
        for (int i = 0; i < 28; ++i) {
#pragma unroll
            for (int ct = 0; ct < 4; ++ct) acc[i & 1][ct] = mfma(W[i >> 1][ct], asb8(B[i]), acc[i & 1][ct]);
            if (i + F2D < 28) ld(i + F2D);
            else if (i + F2D < 30) hb[i + F2D - 28] = gld64(rs, WS_HB + go[i + F2D - 28], s * 40960);
            if (i == 16) fv = ald32(rs, w == 0 ? ((l & 15) < 14 ? l & 15 : 0) * 4 : 0x80000000u, fb_ + C_F1P);
            if (i == 20 && pre) __builtin_amdgcn_s_barrier();
            SB();
        }
        const u32 lb = 0;
#pragma unroll
        for (int i = 0; i < 8; ++i) lds_w128(lb + w * 8704 + ((i >> 2) * 16 + (l & 15)) * 272 + (i & 3) * 64 + g * 16, __builtin_bit_cast(v4u, acc[i >> 2][i & 3]));
        WAIT_VR(0, fv);
        fv = min(fv, roru<1>(fv)); fv = min(fv, roru<2>(fv)); fv = min(fv, roru<4>(fv)); fv = min(fv, roru<8>(fv));
        if (t == 0) *(LAS u32*)(uintptr_t)34816 = fv;
        TACC(2); BAR(); TACC(4);
        f4 x[2][4];
#pragma unroll
        for (int r = 0; r < 2; ++r)
#pragma unroll
            for (int j = 0; j < 4; ++j) x[r][j] = __builtin_bit_cast(f4, lds_r128(lb + ro[r] + j * 8704));
        pre = __builtin_amdgcn_readfirstlane(*(LAS u32*)(uintptr_t)34816) > (u32)s + 1;
        if (pre) TCNT(7);
        SB();
#pragma unroll
        for (int r = 0; r < 2; ++r) {
            const f4 y = (x[r][0] + x[r][1]) + (x[r][2] + x[r][3]);
            const u32 y0 = pk2(y[0], y[1]), y1 = pk2(y[2], y[3]);
            const v2u o = {pk2(asf(hb[r][0] << 16) + asf(y0 << 16), asf(hb[r][0] & 0xffff0000u) + asf(y0 & 0xffff0000u)),
                           pk2(asf(hb[r][1] << 16) + asf(y1 << 16), asf(hb[r][1] & 0xffff0000u) + asf(y1 & 0xffff0000u))};
            *(v2u*)((unsigned char*)a.out + (size_t)s * 40960 + go[r]) = o;
        }
        TSTAMP(246 + idx, s, t); TACC(3);
    }
    TOUT(246 + idx, t);
}

__global__ void __launch_bounds__(256, 1) k_main(Args a) {
    const int t = threadIdx.x, l = t & 63, w = __builtin_amdgcn_readfirstlane(t >> 6);
    const rsrc_t rs = __builtin_amdgcn_make_buffer_rsrc(a.ws, 0, 0x7FFFFFFF, 0x00020000);
    const u32 xx = blockIdx.x & 7, tk = blockIdx.x >> 3;
    const int role = xx < 4 ? xx * 32 + tk : 128 + tk * 4 + (xx - 4);
    const int hd = HPERM ? (role & 3) * 8 + ((role >> 2) & 7) : role & 31;
    if (role < 128) chain_wave(a, role, w, l);
    if (role < 160) { kv_role(a, rs, hd, w, l); __builtin_amdgcn_endpgm(); }
    if (role < 192) { qa_role(a, rs, hd, w, l); __builtin_amdgcn_endpgm(); }
    const u32 fb_ = ln_part(a, rs, role - 192, w, l);
    if (role < 224) { wo_role(a, rs, fb_, role - 192, w, l); __builtin_amdgcn_endpgm(); }
    if (role < 232) { red_role(a, rs, fb_, role - 224, w, l); __builtin_amdgcn_endpgm(); }
    if (role < 246) { f1_role(a, rs, fb_, role - 232, w, l); __builtin_amdgcn_endpgm(); }
    f2_role(a, rs, fb_, role - 246, w, l);
}

extern "C" void kernel_launch(void* const* d_in, const int* in_sizes, int n_in, void* d_out, int out_size, void* d_ws, size_t ws_size, hipStream_t stream) {
    Args a;
    a.x = (const u16*)d_in[0]; a.mem0 = (const u16*)d_in[1]; a.Wq = (const u16*)d_in[2]; a.Wk = (const u16*)d_in[3]; a.Wv = (const u16*)d_in[4]; a.Wo = (const u16*)d_in[5];
    a.w1 = (const u16*)d_in[8]; a.w2 = (const u16*)d_in[9]; a.out = (u16*)d_out; a.dl = g_dl; a.pad = 0;
    a.ws = (unsigned char*)(((uintptr_t)d_ws + (2u << 20) - 1) & ~(uintptr_t)((2u << 20) - 1));
    hipFuncSetAttribute((const void*)k_main, hipFuncAttributeMaxDynamicSharedMemorySize, 163840);
    k_pro<<<132, 1024, 0, stream>>>(a);
    k_main<<<256, 256, 163840, stream>>>(a);
}
```
